```python
import torch

def _softmax(x, axis=-1):
    return torch.softmax(x, dim=axis)

def _gelu(x):
    return torch.nn.functional.gelu(x, approximate='tanh')

def _silu(x):
    return torch.nn.functional.silu(x)

def _sigmoid(x):
    return torch.sigmoid(x)

def _layernorm(x, axis=-1, eps=1e-05):
    m = x.mean(dim=axis, keepdim=True)
    v = x.var(dim=axis, keepdim=True, unbiased=False)
    return (x - m) / torch.sqrt(v + eps)

def _scatter_add(tgt, idx, src, axis=0):
    return tgt.clone().index_add_(axis, idx, src)

def _segment_sum(src, seg, n):
    out = torch.zeros((n,) + src.shape[1:], dtype=src.dtype)
    return out.index_add_(0, seg, src)

def _pad(x, pw):
    flat = tuple((v for pair in reversed(pw) for v in pair))
    return torch.nn.functional.pad(x, flat)
kernel_name = 'interleave_layers__sliding_local_dilated__mixer_gmlp__prompt'

def setup_inputs():

    _w = lambda a: (lambda t: t.to(torch.bfloat16) if t.is_floating_point() else t)(torch.as_tensor(a))

    def _A_setup():
        B, S, D = (1, 2048, 640)
        return {'x': _w((lambda *s: torch.randn(*s, dtype=torch.bfloat16) * 0.05)(B, S, D)), 'wqkv': _w((lambda *s: torch.randn(*s, dtype=torch.bfloat16) * 0.1)(D, 3 * D)), 'wo': _w((lambda *s: torch.randn(*s, dtype=torch.bfloat16) * 0.1)(D, D))}
    _A_in = _A_setup()

    def _B_setup():
        B, S, D, F = (1, 2048, 640, 1728)
        Fh = F // 2
        return {'x': _w((lambda *s: torch.randn(*s, dtype=torch.bfloat16) * 0.05)(B, S, D)), 'w_in': _w((lambda *s: torch.randn(*s, dtype=torch.bfloat16) * 0.8)(D, 2 * Fh)), 'w_sp': _w(0.01 * (lambda *s: torch.randn(*s, dtype=torch.bfloat16) * 0.8)(S, S)), 'b_sp': _w(1.0 + 0.01 * (lambda *s: torch.randn(*s, dtype=torch.bfloat16) * 0.05)(S)), 'w_out': _w((lambda *s: torch.randn(*s, dtype=torch.bfloat16) * 0.05)(Fh, D))}
    _B_in = _B_setup()
    return {'A_x': _A_in['x'], 'A_wqkv': _A_in['wqkv'], 'A_wo': _A_in['wo'], 'B_x': _B_in['x'], 'B_w_in': _B_in['w_in'], 'B_w_sp': _B_in['w_sp'], 'B_b_sp': _B_in['b_sp'], 'B_w_out': _B_in['w_out']}

def reference(A_x, A_wqkv, A_wo, B_x, B_w_in, B_w_sp, B_b_sp, B_w_out):

    def _A_ref(x, wqkv, wo):
        B, S, D = x.shape
        H = 32
        Dh = D // H
        W, dil = (8, 2)
        qkv = (lambda x, *d: x.permute(*d))((x @ wqkv).reshape(B, S, 3, H, Dh), 2, 0, 3, 1, 4)
        q, k, v = (qkv[0], qkv[1], qkv[2])
        scale = Dh ** (-0.5)
        pos = torch.arange(S)
        off = torch.arange(W) * dil
        idx = pos[:, None] - off[None, :]
        mask = idx >= 0
        idx_c = torch.where(mask, idx, 0).reshape(-1)
        kg = k[:, :, idx_c, :].reshape(B, H, S, W, Dh)
        vg = v[:, :, idx_c, :].reshape(B, H, S, W, Dh)
        logits = torch.einsum('bhsd,bhswd->bhsw', q, kg) * scale
        logits = torch.where(mask[None, None, :, :], logits, -1000000000.0)
        attn = _softmax(logits, axis=-1)
        o = torch.einsum('bhsw,bhswd->bhsd', attn, vg)
        return (lambda x, *d: x.permute(*d))(o, 0, 2, 1, 3).reshape(B, S, D) @ wo

    def _B_ref(x, w_in, w_sp, b_sp, w_out):
        B, S, D = x.shape
        F = w_in.shape[-1]
        Fh = F // 2
        z = _gelu(torch.einsum('bsd,df->bsf', x, w_in))
        z1 = z[..., :Fh].clone()
        z2 = _layernorm(z[..., Fh:], axis=-1)
        g = torch.einsum('ts,bsf->btf', w_sp, z2) + b_sp[None, :, None]
        return torch.einsum('bsf,fd->bsd', z1 * g, w_out)
    x = A_x
    x = x + _A_ref(_layernorm(x, axis=-1), A_wqkv, A_wo)
    x = x + _B_ref(_layernorm(x, axis=-1), B_w_in, B_w_sp, B_b_sp, B_w_out)
    x = x + _A_ref(_layernorm(x, axis=-1), A_wqkv, A_wo)
    x = x + _B_ref(_layernorm(x, axis=-1), B_w_in, B_w_sp, B_b_sp, B_w_out)
    return x
```

```cpp
#include <hip/hip_runtime.h>
#include <hip/hip_bf16.h>
#include <stdint.h>

typedef short bf16x8 __attribute__((ext_vector_type(8)));
typedef float f32x4 __attribute__((ext_vector_type(4)));
typedef unsigned short u16;

__device__ __forceinline__ float bf2f(u16 b) { return __builtin_bit_cast(float, (unsigned)b << 16); }
__device__ __forceinline__ u16 f2bf(float f) {
  unsigned u = __builtin_bit_cast(unsigned, f);
  u += 0x7FFFu + ((u >> 16) & 1u);
  return (u16)(u >> 16);
}

constexpr int S = 2048, D = 640, H = 32, DH = 20, F = 1728, FH = 864;

__global__ void ln_kernel(const u16* in, int ld, u16* out, int ldo, int n) {
  int row = blockIdx.x;
  int t = threadIdx.x;
  float s = 0.f, ss = 0.f;
  for (int i = t; i < n; i += 64) { float v = bf2f(in[(size_t)row * ld + i]); s += v; }
  for (int o = 32; o; o >>= 1) s += __shfl_xor(s, o);
  float m = s / n;
  for (int i = t; i < n; i += 64) { float v = bf2f(in[(size_t)row * ld + i]) - m; ss += v * v; }
  for (int o = 32; o; o >>= 1) ss += __shfl_xor(ss, o);
  float r = rsqrtf(ss / n + 1e-5f);
  for (int i = t; i < n; i += 64) out[(size_t)row * ldo + i] = f2bf((bf2f(in[(size_t)row * ld + i]) - m) * r);
}

template <int MODE>
__global__ void __launch_bounds__(64) gemm_kernel(const u16* A, int lda, const u16* B, int ldb, u16* C, int ldc, int K,
                                                  const u16* aux, int ldaux, const u16* bias) {
  __shared__ u16 sA[16][40];
  __shared__ u16 sB[32][24];
  int bx = blockIdx.x * 16, by = blockIdx.y * 16;
  int lane = threadIdx.x, r = lane & 15, q = lane >> 4;
  f32x4 acc = {0, 0, 0, 0};
  for (int k0 = 0; k0 < K; k0 += 32) {
    for (int e = lane; e < 512; e += 64) {
      sA[e / 32][e % 32] = A[(size_t)(by + e / 32) * lda + k0 + e % 32];
      sB[e / 16][e % 16] = B[(size_t)(k0 + e / 16) * ldb + bx + e % 16];
    }
    __syncthreads();
    bf16x8 a, b;
    for (int j = 0; j < 8; ++j) { a[j] = sA[r][q * 8 + j]; b[j] = sB[q * 8 + j][r]; }
    acc = __builtin_amdgcn_mfma_f32_16x16x32_bf16(a, b, acc, 0, 0, 0);
    __syncthreads();
  }
  for (int j = 0; j < 4; ++j) {
    int row = by + q * 4 + j, col = bx + r;
    float v = acc[j];
    if (MODE == 1) v = bf2f(f2bf(v)) + bf2f(aux[(size_t)row * ldaux + col]);
    if (MODE == 2) { v = bf2f(f2bf(v)); float u = 0.7978845608028654f * (v + 0.044715f * v * v * v); v = 0.5f * v * (1.f + tanhf(u)); }
    if (MODE == 3) { v = bf2f(f2bf(bf2f(f2bf(v)) + bf2f(bias[row]))) * bf2f(aux[(size_t)row * ldaux + col]); }
    C[(size_t)row * ldc + col] = f2bf(v);
  }
}

__global__ void attn_kernel(const u16* qkv, u16* o) {
  int idx = blockIdx.x * blockDim.x + threadIdx.x;
  int s = idx / H, h = idx % H;
  float q[DH];
  for (int d = 0; d < DH; ++d) q[d] = bf2f(qkv[(size_t)s * 1920 + h * DH + d]);
  float lg[8], mx = -1e30f;
  for (int w = 0; w < 8; ++w) {
    int p = s - 2 * w;
    if (p >= 0) {
      float a = 0.f;
      for (int d = 0; d < DH; ++d) a += q[d] * bf2f(qkv[(size_t)p * 1920 + 640 + h * DH + d]);
      lg[w] = a * 0.22360679774997896f;
    } else lg[w] = -1e9f;
    mx = fmaxf(mx, lg[w]);
  }
  float sum = 0.f;
  for (int w = 0; w < 8; ++w) { lg[w] = __expf(lg[w] - mx); sum += lg[w]; }
  float inv = 1.f / sum;
  for (int d = 0; d < DH; ++d) {
    float a = 0.f;
    for (int w = 0; w < 8; ++w) { int p = s - 2 * w; if (p < 0) p = 0; a += lg[w] * inv * bf2f(qkv[(size_t)p * 1920 + 1280 + h * DH + d]); }
    o[(size_t)s * D + h * DH + d] = f2bf(a);
  }
}

extern "C" void kernel_launch(void* const* d_in, const int* in_sizes, int n_in, void* d_out, int out_size, void* d_ws,
                              size_t ws_size, hipStream_t stream) {
  const u16* x0 = (const u16*)d_in[0];
  const u16* wqkv = (const u16*)d_in[1];
  const u16* wo = (const u16*)d_in[2];
  const u16* w_in = (const u16*)d_in[4];
  const u16* w_sp = (const u16*)d_in[5];
  const u16* b_sp = (const u16*)d_in[6];
  const u16* w_out = (const u16*)d_in[7];
  u16* ws = (u16*)d_ws;
  u16* xn = ws;
  u16* qkv = xn + S * D;
  u16* o = qkv + S * 1920;
  u16* x1 = o + S * D;
  u16* z = x1 + S * D;
  u16* z2n = z + S * F;
  u16* u = z2n + S * FH;
  u16* x2 = u + S * FH;
  const u16* x = x0;
  for (int l = 0; l < 2; ++l) {
    ln_kernel<<<S, 64, 0, stream>>>(x, D, xn, D, D);
    gemm_kernel<0><<<dim3(1920 / 16, S / 16), 64, 0, stream>>>(xn, D, wqkv, 1920, qkv, 1920, D, nullptr, 0, nullptr);
    attn_kernel<<<S * H / 256, 256, 0, stream>>>(qkv, o);
    gemm_kernel<1><<<dim3(D / 16, S / 16), 64, 0, stream>>>(o, D, wo, D, x1, D, D, x, D, nullptr);
    ln_kernel<<<S, 64, 0, stream>>>(x1, D, xn, D, D);
    gemm_kernel<2><<<dim3(F / 16, S / 16), 64, 0, stream>>>(xn, D, w_in, F, z, F, D, nullptr, 0, nullptr);
    ln_kernel<<<S, 64, 0, stream>>>(z + FH, F, z2n, FH, FH);
    gemm_kernel<3><<<dim3(FH / 16, S / 16), 64, 0, stream>>>(w_sp, S, z2n, FH, u, FH, S, z, F, b_sp);
    u16* xo = (l == 0) ? x2 : (u16*)d_out;
    gemm_kernel<1><<<dim3(D / 16, S / 16), 64, 0, stream>>>(u, FH, w_out, D, xo, D, FH, x1, D, nullptr);
    x = x2;
  }
}
```

```cpp
#include <hip/hip_runtime.h>
#include <stdint.h>

typedef unsigned short u16;
typedef unsigned int u32;
typedef unsigned long long u64;
typedef u32 v4u __attribute__((ext_vector_type(4)));
typedef u32 v2u __attribute__((ext_vector_type(2)));
typedef float f32x4 __attribute__((ext_vector_type(4)));
typedef float f32x2 __attribute__((ext_vector_type(2)));
typedef __bf16 bf16x8 __attribute__((ext_vector_type(8)));
typedef __bf16 bf16x2 __attribute__((ext_vector_type(2)));
typedef short s16x4 __attribute__((ext_vector_type(4)));
#define GAS __attribute__((address_space(1)))
#define LAS __attribute__((address_space(3)))

#define STAMPS 0

namespace {

constexpr int S = 2048, D = 640, FH = 864;
constexpr u32 OFF_CNT = 0;
constexpr u32 OFF_ST = 65536;
constexpr u32 ZERO_BYTES = OFF_ST + 2 * 32 * 8 * 64 * 16;
constexpr u32 OFF_CQ = ZERO_BYTES;
constexpr u32 OFF_CI = OFF_CQ + 8192;
constexpr u32 OFF_WQ = OFF_CI + 8192;
constexpr u32 OFF_WO = OFF_WQ + 8 * 16 * 20 * 1024;
constexpr u32 OFF_WI = OFF_WO + 8 * 5 * 20 * 1024;
constexpr u32 OFF_WOUT = OFF_WI + 8 * 16 * 20 * 1024;
constexpr u32 OFF_WSP = OFF_WOUT + 8 * 5 * 27 * 1024;
constexpr u32 AT20 = 128 * 20 * 1024, AT27 = 128 * 27 * 1024, ZSZ = 8 * 7 * 64 * 1024;
constexpr u32 XSZ = AT20 + 20 * 1024;
constexpr u32 OFF_X = OFF_WSP + 32 * 8 * 32 * 1024 + 20 * 1024;
constexpr u32 OFF_O = OFF_X + XSZ + AT20;
constexpr u32 OFF_X1 = OFF_O + 2 * AT20;
constexpr u32 OFF_Z2 = OFF_X1 + 2 * AT20;
constexpr u32 OFF_U = OFF_Z2 + 2 * ZSZ;
constexpr u32 OFF_DBG = OFF_U + 2 * AT27;
constexpr u32 WS_BYTES = OFF_DBG + 256 * 64 * 8;
constexpr u32 C_O = 0, C_X1 = 64, C_U = 128, C_X2 = 192, C_Z = 224, C_ERR = 352;

constexpr u32 L_XT = 0;
constexpr u32 L_QKV = 102400;
constexpr u32 QKV_STRIDE = 488;
constexpr u32 L_EXCH = 138240;
constexpr u32 L_PART = 81920;
constexpr u32 L_FIN = 86016;
constexpr u32 L_TR = 88064;
constexpr u32 L_Z1 = 143360;
constexpr u32 L_STAT = 158720;
constexpr u32 L_TS = 159744;
constexpr u32 LDS_BYTES = 160 * 1024;

constexpr int SC1 = 16;
constexpr u32 SPIN_CAP = 1u << 16;

__device__ __forceinline__ float bf2f(u32 lo16) { return __builtin_bit_cast(float, lo16 << 16); }
__device__ __forceinline__ float bfhi(u32 x) { return __builtin_bit_cast(float, x & 0xffff0000u); }
__device__ __forceinline__ u32 pack2(float a, float b) {
  f32x2 v = {a, b};
  return __builtin_bit_cast(u32, __builtin_convertvector(v, bf16x2));
}
__device__ __forceinline__ f32x4 mfma(v4u a, v4u b, f32x4 c) {
  return __builtin_amdgcn_mfma_f32_16x16x32_bf16(__builtin_bit_cast(bf16x8, a), __builtin_bit_cast(bf16x8, b), c, 0, 0, 0);
}
__device__ __forceinline__ float dot2(u32 a, u32 b, float c) {
  return __builtin_amdgcn_fdot2_f32_bf16(__builtin_bit_cast(bf16x2, a), __builtin_bit_cast(bf16x2, b), c, false);
}
__device__ __forceinline__ float fbits(u32 x) { return __builtin_bit_cast(float, x); }
__device__ __forceinline__ u32 ubits(float x) { return __builtin_bit_cast(u32, x); }
__device__ __forceinline__ float xsum32(float x) {
  auto r = __builtin_amdgcn_permlane32_swap(ubits(x), ubits(x), false, false);
  return fbits(r[0]) + fbits(r[1]);
}
__device__ __forceinline__ float xsum16(float x) {
  auto r = __builtin_amdgcn_permlane16_swap(ubits(x), ubits(x), false, false);
  return fbits(r[0]) + fbits(r[1]);
}
__device__ __forceinline__ float gelu(float x) {
  const float t = __builtin_fmaf(x * x, -0.10294324f, -2.30220820f);
  const float e = __builtin_amdgcn_exp2f(x * t);
  return x * __builtin_amdgcn_rcpf(1.0f + e);
}

__device__ __forceinline__ void st128(v4u v, __amdgpu_buffer_rsrc_t rs, u32 voff, u32 soff) {
  __builtin_amdgcn_raw_buffer_store_b128(v, rs, voff + soff, 0, SC1);
}
#define WAITV(n) asm volatile("s_waitcnt vmcnt(%0)" ::"n"(n) : "memory")
#define BAR() asm volatile("s_waitcnt lgkmcnt(0)\n\ts_barrier" ::: "memory")
#define SCHED() __builtin_amdgcn_sched_barrier(0)

template <bool COH>
__device__ __forceinline__ void dma16(v4u srd, u32 voff, u32 soff, u32 ldsoff) {
  if (COH)
    asm volatile("s_mov_b32 m0, %0\n\ts_nop 0\n\tbuffer_load_dwordx4 %1, %2, %3 offen sc1 lds" ::"s"(ldsoff), "v"(voff), "s"(srd), "s"(soff) : "memory");
  else
    asm volatile("s_mov_b32 m0, %0\n\ts_nop 0\n\tbuffer_load_dwordx4 %1, %2, %3 offen lds" ::"s"(ldsoff), "v"(voff), "s"(srd), "s"(soff) : "memory");
}

__host__ __device__ constexpr int d_pair(int m, int P) { return 80 * m + ((m & 1) ? 16 : 0) + 32 * P; }
__host__ __device__ constexpr int d_single(int m) { return 80 * m + ((m & 1) ? 0 : 64); }
__host__ __device__ constexpr int f_base(int m) { return m < 7 ? 112 * m : 768; }
__host__ __device__ constexpr int f_pair(int m, int P) { return f_base(m) + (((m & 1) && m < 6) ? 16 : 0) + 32 * P; }
__host__ __device__ constexpr int f_single(int m) { return f_base(m) + ((m & 1) ? 0 : 96); }

struct PrepArgs {
  const u16 *x, *wqkv, *wo, *win, *wsp, *wout;
  char* ws;
};

__device__ __forceinline__ int qkv_col(int m, int slot, int s) {
  const int w = slot >> 1, j = slot & 1;
  int kv = -1, q = -1;
  if (j == 0) kv = w;
  else if (w == 2) kv = 8;
  else if (w == 3) kv = 9;
  else if (w == 7) kv = 7;
  else q = (w < 2) ? w : w - 2;
  if (q >= 0) return 80 * m + 16 * q + s;
  return (kv < 5) ? 640 + 80 * m + 16 * kv + s : 1280 + 80 * m + 16 * (kv - 5) + s;
}
__device__ __forceinline__ int dout_col(int m, int ct, int s) {
  if (ct < 4) return d_pair(m, ct >> 1) + 8 * (s >> 2) + 4 * (ct & 1) + (s & 3);
  return d_single(m) + s;
}
__device__ __forceinline__ int f_col(int m, int ct, int s) {
  if (ct < 6) return f_pair(m, ct >> 1) + 8 * (s >> 2) + 4 * (ct & 1) + (s & 3);
  return (m < 6) ? f_single(m) + s : -1;
}
__device__ __forceinline__ int o_row(int k) {
  const int id = k >> 3, j = k & 7, m = id / 10, c = id % 10;
  if (c < 8) return 80 * m + 20 * (c >> 1) + 8 * (c & 1) + j;
  return 80 * m + 20 * (2 * (c - 8) + (j >> 2)) + 16 + (j & 3);
}

constexpr int PB_WQ = 0, PB_WI = 128, PB_WO = 256, PB_WOUT = 296, PB_WSP = 336, PB_X = 592, PB_ZERO = 672, PB_ZX = 708, PB_END = 710;

__global__ void __launch_bounds__(256) prep_kernel(PrepArgs a) {
  __shared__ float red[4][16];
  const int b = blockIdx.x, tid = threadIdx.x, lane = tid & 63, wv = tid >> 6;
  const int s = lane & 15, kq = lane >> 4;
  if (b < PB_WSP) {
    const u16* src;
    int ld, col, nks, kind;
    u32 dst;
    float* cs = nullptr;
    if (b < PB_WI) {
      const int m = b >> 4, slot = b & 15;
      src = a.wqkv; ld = 1920; col = qkv_col(m, slot, s); nks = 20; kind = 0;
      dst = OFF_WQ + b * 20 * 1024; cs = (float*)(a.ws + OFF_CQ) + b * 16;
    } else if (b < PB_WO) {
      const int bb = b - PB_WI, m = bb >> 4, slot = bb & 15, ct = slot >> 1;
      src = a.win; ld = 1728; nks = 20; kind = 0;
      col = (ct < 7) ? f_col(m, ct, s) : -1;
      if (col >= 0 && !(slot & 1)) col += FH;
      dst = OFF_WI + bb * 20 * 1024; cs = (float*)(a.ws + OFF_CI) + bb * 16;
    } else if (b < PB_WOUT) {
      const int bb = b - PB_WO;
      src = a.wo; ld = 640; col = dout_col(bb / 5, bb % 5, s); nks = 20; kind = 1;
      dst = OFF_WO + bb * 20 * 1024;
    } else {
      const int bb = b - PB_WOUT;
      src = a.wout; ld = 640; col = dout_col(bb / 5, bb % 5, s); nks = 27; kind = 0;
      dst = OFF_WOUT + bb * 27 * 1024;
    }
    float sum = 0.f;
#pragma unroll
    for (int i = 0; i < 7; ++i) {
      const int ks = wv + 4 * i;
      if (ks < nks) {
        u32 e[8];
#pragma unroll
        for (int j = 0; j < 8; ++j) {
          int k = 32 * ks + 8 * kq + j;
          if (kind == 1) k = o_row(k);
          e[j] = (col >= 0) ? (u32)src[(size_t)k * ld + col] : 0u;
        }
#pragma unroll
        for (int j = 0; j < 8; ++j) sum += bf2f(e[j]);
        v4u v = {e[0] | (e[1] << 16), e[2] | (e[3] << 16), e[4] | (e[5] << 16), e[6] | (e[7] << 16)};
        *(v4u*)(a.ws + dst + ks * 1024 + lane * 16) = v;
      }
    }
    if (cs) {
      sum = xsum32(xsum16(sum));
      if (lane < 16) red[wv][lane] = sum;
      __syncthreads();
      if (tid < 16) cs[tid] = (red[0][tid] + red[1][tid]) + (red[2][tid] + red[3][tid]);
    }
  } else if (b < PB_X) {
    const int bb = b - PB_WSP, p = bb >> 3, w = bb & 7;
#pragma unroll
    for (int ks = 0; ks < 8; ++ks) {
      const v4u v = *(const v4u*)(a.wsp + (size_t)(64 * p + 16 * wv + s) * S + 256 * w + 32 * ks + 8 * kq);
      *(v4u*)(a.ws + OFF_WSP + (bb * 32 + ks * 4 + wv) * 1024 + lane * 16) = v;
    }
  } else if (b < PB_ZERO) {
    const int bb = b - PB_X;
#pragma unroll
    for (int i = 0; i < 8; ++i) {
      const int f = bb * 32 + wv * 8 + i, rt = f / 20, ks = f % 20;
      const v4u v = *(const v4u*)(a.x + (size_t)(16 * rt + s) * D + 32 * ks + 8 * kq);
      *(v4u*)(a.ws + OFF_X + f * 1024 + lane * 16) = v;
    }
  } else if (b >= PB_ZX) {
#pragma unroll
    for (int i = 0; i < 5; ++i) *(v4u*)(a.ws + OFF_X + (b - PB_ZX) * XSZ - 20 * 1024 + i * 4096 + tid * 16) = v4u{0u, 0u, 0u, 0u};
  } else {
    const int bb = b - PB_ZERO;
#pragma unroll
    for (int i = 0; i < 4; ++i) *(v4u*)(a.ws + (bb * 4 + i) * 4096 + tid * 16) = v4u{0u, 0u, 0u, 0u};
  }
}
static_assert((PB_ZX - PB_ZERO) * 16384 == ZERO_BYTES, "zero range");

struct MegaArgs {
  char* ws;
  const u16* bsp;
  u16* out;
};

__device__ __forceinline__ void wait_count(GAS u32* cnt, u32 idx, u32 want) {
  u32 spins = 0;
#pragma unroll 1
  while (__hip_atomic_load(cnt + idx * 32, __ATOMIC_RELAXED, __HIP_MEMORY_SCOPE_AGENT) < want) {
    if (++spins > SPIN_CAP) {
      __hip_atomic_store(cnt + C_ERR * 32, 1u, __ATOMIC_RELAXED, __HIP_MEMORY_SCOPE_AGENT);
      break;
    }
  }
  __builtin_amdgcn_fence(__ATOMIC_ACQUIRE, "wavefront");
}
__device__ __forceinline__ void signal(GAS u32* cnt, u32 idx) {
  __hip_atomic_fetch_add(cnt + idx * 32, 1u, __ATOMIC_RELAXED, __HIP_MEMORY_SCOPE_AGENT);
}

#define TS(i) do {} while (0)

#define LDW(off_s, i, aux) __builtin_amdgcn_raw_buffer_load_b128(rs, lane16 + (((i) & 3) << 10), (off_s) + (((i) >> 2) << 12), (aux))

__device__ __forceinline__ void ln_stats(LAS char* L, int rt, int lane) {
  float s0 = 0.f, s1 = 0.f, q0 = 0.f, q1 = 0.f;
#pragma unroll
  for (int ks = 0; ks < 20; ++ks) {
    const v4u a = *(LAS v4u*)(L + L_XT + (rt * 20 + ks) * 1024 + lane * 16);
    s0 = dot2(a.x, 0x3f803f80u, s0); q0 = dot2(a.x, a.x, q0);
    s1 = dot2(a.y, 0x3f803f80u, s1); q1 = dot2(a.y, a.y, q1);
    s0 = dot2(a.z, 0x3f803f80u, s0); q0 = dot2(a.z, a.z, q0);
    s1 = dot2(a.w, 0x3f803f80u, s1); q1 = dot2(a.w, a.w, q1);
  }
  const float s = xsum32(xsum16(s0 + s1)), q = xsum32(xsum16(q0 + q1));
  const float mean = s * (1.0f / D);
  const float var = __builtin_fmaxf(q * (1.0f / D) - mean * mean, 0.f);
  if (lane < 16) *(LAS f32x2*)(L + L_STAT + (rt * 16 + lane) * 8) = f32x2{mean, __builtin_amdgcn_rsqf(var + 1e-5f)};
}

constexpr int E1 = 14;
constexpr int E3A = 12;
constexpr int E3B = 28;
constexpr int E4A = 20;
constexpr int RING = 12;

__global__ void __launch_bounds__(512, 2) mega_kernel(MegaArgs a) {
  extern __shared__ __attribute__((aligned(16))) char smem[];
  LAS char* L = (LAS char*)smem;
  const u32 lds0 = (u32)(uintptr_t)L;
  int tid = threadIdx.x, w = __builtin_amdgcn_readfirstlane(tid >> 6);
  const int b = blockIdx.x;
  int p = (b & 7) * 4 + (b >> 6), m = (b >> 3) & 7;
  int lane, q, t, r, kh, nct;
  u32 lane16;
#define REFRESH() do { asm volatile("" : "+v"(tid)); asm volatile("" : "+s"(w), "+s"(p), "+s"(m)); lane = tid & 63; lane16 = lane * 16; q = lane >> 4; t = lane & 15; \
    r = w & 3; kh = w >> 2; nct = m < 6 ? 7 : 6; } while (0)
  REFRESH();
  const __amdgpu_buffer_rsrc_t rs = __builtin_amdgcn_make_buffer_rsrc((void*)a.ws, 0, (int)WS_BYTES, 0x00020000);
  const __amdgpu_buffer_rsrc_t rs_out = __builtin_amdgcn_make_buffer_rsrc((void*)a.out, 0, S * D * 2, 0x00020000);
  const v4u srd = {(u32)(uintptr_t)a.ws, (u32)((uintptr_t)a.ws >> 32) & 0xffffu, WS_BYTES, 0x00020000u};
  GAS u32* cnt = (GAS u32*)(a.ws + OFF_CNT);
  v4u W[40];
#define wq0 (OFF_WQ + (m * 16 + 2 * w) * 20 * 1024)
#define wq1 (wq0 + 20 * 1024)
#pragma unroll
  for (int i = 0; i < E1; ++i) W[i] = (i & 1) ? LDW(wq1, i >> 1, 0) : LDW(wq0, i >> 1, 0);
  SCHED();

#pragma unroll 1
  for (int it = 0; it < 2; ++it) {
    const u32 xin = OFF_X + it * XSZ;
    TS(0);
    REFRESH();
    u32 o5[5];
#pragma unroll
    for (int i = 0; i < 5; ++i) {
      const int n = w + 8 * i, ks = (n * 205) >> 10;
      o5[i] = ((n - 5 * ks) * 20 + ks) * 1024;
      asm volatile("" : "+s"(o5[i]));
    }
    u32 xg = xin + (4 * p - 1) * 20 * 1024;
    asm volatile("" : "+s"(xg));
    if (it == 1) {
      if (w == 0) {
        wait_count(cnt, C_X2 + p, 8);
        if (p > 0) wait_count(cnt, C_X2 + p - 1, 8);
      }
      BAR();
    }
    TS(1);
    REFRESH();
#pragma unroll
    for (int i = 0; i < 13; ++i) {
      u32 o = o5[i % 5] + (i / 5) * 8192;
      if (i == 12) o = w < 4 ? o : o5[1] + 2 * 8192;
      dma16<true>(srd, lane16, xg + o, lds0 + L_XT + o);
    }
    SCHED();
#pragma unroll
    for (int i = E1; i < 40; ++i) W[i] = (i & 1) ? LDW(wq1, i >> 1, 0) : LDW(wq0, i >> 1, 0);
    v4u cs0u = __builtin_amdgcn_raw_buffer_load_b128(rs, q * 16, OFF_CQ + (m * 16 + 2 * w) * 64, 0);
    v4u cs1u = __builtin_amdgcn_raw_buffer_load_b128(rs, q * 16 + 64, OFF_CQ + (m * 16 + 2 * w) * 64, 0);
    SCHED();
    {
      f32x4 acc[2][5];
#pragma unroll
      for (int j = 0; j < 2; ++j)
#pragma unroll
        for (int rt = 0; rt < 5; ++rt) acc[j][rt] = f32x4{0.f, 0.f, 0.f, 0.f};
#pragma unroll
      for (int ks = 0; ks < 20; ++ks) {
        if (ks == 0) { WAITV(42 - E1 + 11); BAR(); TS(2); }
        if (ks == 3) { WAITV(42 - E1 + 8); BAR(); }
        if (ks == 8) {
          WAITV(42 - E1); BAR();
          if (w < 5) ln_stats(L, w, lane);
        }
#pragma unroll
        for (int rt = 0; rt < 5; ++rt) {
          const v4u x = *(LAS v4u*)(L + L_XT + (rt * 20 + ks) * 1024 + lane16);
          acc[0][rt] = mfma(W[2 * ks], x, acc[0][rt]);
          acc[1][rt] = mfma(W[2 * ks + 1], x, acc[1][rt]);
        }
      }
      asm volatile("" : "+v"(cs0u), "+v"(cs1u));
      SCHED();
      BAR();
      SCHED();
      TS(3);
    REFRESH();
#pragma unroll
      for (int i = 0; i < 13; ++i) {
        int n = w + 8 * i;
        n = n > 99 ? 99 : n;
        dma16<false>(srd, lane16, OFF_WO + (m * 100 + n) * 1024, lds0 + L_XT + n * 1024);
      }
      const f32x4 cs[2] = {__builtin_bit_cast(f32x4, cs0u), __builtin_bit_cast(f32x4, cs1u)};
      const int col0 = 80 + 16 * w;
      const int col1 = (int)((0xC432ED10u >> (4 * w)) & 15u) * 16;
#pragma unroll
      for (int rt = 0; rt < 5; ++rt) {
        const f32x2 st = *(LAS f32x2*)(L + L_STAT + (rt * 16 + t) * 8);
#pragma unroll
        for (int j = 0; j < 2; ++j) {
          f32x4 v;
#pragma unroll
          for (int i = 0; i < 4; ++i) v[i] = st.y * __builtin_fmaf(-st.x, cs[j][i], acc[j][rt][i]);
          *(LAS v2u*)(L + L_QKV + (rt * 16 + t) * QKV_STRIDE + ((j ? col1 : col0) + 4 * q) * 2) = v2u{pack2(v[0], v[1]), pack2(v[2], v[3])};
        }
      }
    }
    BAR();
    TS(4);
    REFRESH();
    {
      const int h = w & 3, hf = lane >> 5, tok = (w >> 2) * 32 + (lane & 31);
      LAS char* rowq = L + L_QKV + (16 + tok) * QKV_STRIDE + h * 40;
      LAS char* rowk = rowq + 160 - hf * 8 * QKV_STRIDE;
      v2u qv[5];
#pragma unroll
      for (int c = 0; c < 5; ++c) qv[c] = *(LAS v2u*)(rowq + c * 8);
      float lg[4];
#pragma unroll
      for (int i = 0; i < 4; ++i) {
        float s0 = 0.f, s1 = 0.f;
#pragma unroll
        for (int c = 0; c < 5; ++c) {
          const v2u kv = *(LAS v2u*)(rowk - i * 2 * QKV_STRIDE + c * 8);
          s0 = dot2(qv[c].x, kv.x, s0);
          s1 = dot2(qv[c].y, kv.y, s1);
        }
        lg[i] = (s0 + s1) * (0.22360679774997896f * 1.4426950408889634f);
        if (64 * p + tok - 2 * (4 * hf + i) < 0) lg[i] = -1.0e30f;
      }
      float mx = __builtin_fmaxf(__builtin_fmaxf(lg[0], lg[1]), __builtin_fmaxf(lg[2], lg[3]));
      {
        auto rr = __builtin_amdgcn_permlane32_swap(ubits(mx), ubits(mx), false, false);
        mx = __builtin_fmaxf(fbits(rr[0]), fbits(rr[1]));
      }
      float pr[4];
#pragma unroll
      for (int i = 0; i < 4; ++i) pr[i] = __builtin_amdgcn_exp2f(lg[i] - mx);
      const float inv = __builtin_amdgcn_rcpf(xsum32((pr[0] + pr[1]) + (pr[2] + pr[3])));
      const u32 P01 = pack2(pr[0] * inv, pr[1] * inv), P23 = pack2(pr[2] * inv, pr[3] * inv);
      float o[20];
#pragma unroll
      for (int c = 0; c < 5; ++c) {
        const v2u v0 = *(LAS v2u*)(rowk + 160 + c * 8);
        const v2u v1 = *(LAS v2u*)(rowk + 160 - 2 * QKV_STRIDE + c * 8);
        const v2u v2 = *(LAS v2u*)(rowk + 160 - 4 * QKV_STRIDE + c * 8);
        const v2u v3 = *(LAS v2u*)(rowk + 160 - 6 * QKV_STRIDE + c * 8);
        o[4 * c + 0] = dot2(__builtin_amdgcn_perm(v3.x, v2.x, 0x05040100u), P23, dot2(__builtin_amdgcn_perm(v1.x, v0.x, 0x05040100u), P01, 0.f));
        o[4 * c + 1] = dot2(__builtin_amdgcn_perm(v3.x, v2.x, 0x07060302u), P23, dot2(__builtin_amdgcn_perm(v1.x, v0.x, 0x07060302u), P01, 0.f));
        o[4 * c + 2] = dot2(__builtin_amdgcn_perm(v3.y, v2.y, 0x05040100u), P23, dot2(__builtin_amdgcn_perm(v1.y, v0.y, 0x05040100u), P01, 0.f));
        o[4 * c + 3] = dot2(__builtin_amdgcn_perm(v3.y, v2.y, 0x07060302u), P23, dot2(__builtin_amdgcn_perm(v1.y, v0.y, 0x07060302u), P01, 0.f));
      }
      float f[10];
#pragma unroll
      for (int d = 0; d < 8; ++d) {
        auto rr = __builtin_amdgcn_permlane32_swap(ubits(o[d]), ubits(o[8 + d]), false, false);
        f[d] = fbits(rr[0]) + fbits(rr[1]);
      }
#pragma unroll
      for (int d = 0; d < 2; ++d) {
        auto rr = __builtin_amdgcn_permlane32_swap(ubits(o[16 + d]), ubits(o[18 + d]), false, false);
        f[8 + d] = fbits(rr[0]) + fbits(rr[1]);
      }
      const v4u ov = {pack2(f[0], f[1]), pack2(f[2], f[3]), pack2(f[4], f[5]), pack2(f[6], f[7])};
      const u32 lp = pack2(f[8], f[9]);
      auto lr = __builtin_amdgcn_permlane32_swap(lp, lp, false, false);
      const u32 obase = OFF_O + it * AT20 + (4 * p + (tok >> 4)) * 20 * 1024 + (tok & 15) * 16;
      const int id = 10 * m + 2 * h + hf, id2 = 10 * m + 8 + (h >> 1);
      __builtin_amdgcn_raw_buffer_store_b128(ov, rs, obase + id * 256, 0, SC1);
      __builtin_amdgcn_raw_buffer_store_b64(v2u{lr[0], lr[1]}, rs, hf ? 0x80000000u : obase + id2 * 256 + (h & 1) * 8, 0, SC1);
    }
    SCHED();
#define wi0 (OFF_WI + (m * 16 + 2 * w) * 20 * 1024)
    REFRESH();
#pragma unroll
    for (int i = 0; i < E3A; ++i) W[i] = LDW(wi0, i, 0);
#define xoffP (((4 * p + r) * 20 + (d_pair(m, kh) >> 5)) * 1024)
#define xoffS (((4 * p + r) * 20 + (d_single(m) >> 5)) * 1024 + ((d_single(m) & 31) >> 3) * 256)
#define vS ((u32)(((q >> 1) * 16 + t) * 16 + (q & 1) * 8))
    v4u resP = __builtin_amdgcn_raw_buffer_load_b128(rs, lane16, xin + xoffP, SC1);
    v2u resS = __builtin_amdgcn_raw_buffer_load_b64(rs, vS, xin + xoffS, SC1);
    SCHED();
    WAITV(E3A + 2);
    BAR();
    if (tid == 0) signal(cnt, C_O + it * 32 + p);
    TS(5);
    if (w == 0) wait_count(cnt, C_O + it * 32 + p, 8);
    BAR();
    TS(6);
    REFRESH();
    {
      v4u A[10];
      const u32 ao = OFF_O + it * AT20 + ((4 * p + r) * 20 + 10 * kh) * 1024;
#pragma unroll
      for (int i = 0; i < 10; ++i) A[i] = LDW(ao, i, SC1);
      SCHED();
      f32x4 acc[5];
#pragma unroll
      for (int c = 0; c < 5; ++c) acc[c] = f32x4{0.f, 0.f, 0.f, 0.f};
      LAS char* wb = L + L_XT + kh * 10 * 1024 + lane16;
#pragma unroll
      for (int i = 0; i < 10; ++i)
#pragma unroll
        for (int c = 0; c < 5; ++c) acc[c] = mfma(*(LAS v4u*)(wb + (c * 20 + i) * 1024), A[i], acc[c]);
      LAS char* ex = L + L_EXCH + r * 5 * 1024 + lane16;
      if (kh) {
        *(LAS f32x4*)(ex) = acc[0]; *(LAS f32x4*)(ex + 1024) = acc[1]; *(LAS f32x4*)(ex + 4096) = acc[4];
      } else {
        *(LAS f32x4*)(ex + 2048) = acc[2]; *(LAS f32x4*)(ex + 3072) = acc[3];
      }
      BAR();
      TS(7);
    REFRESH();
      const f32x4 a0 = (kh ? acc[2] : acc[0]) + *(LAS f32x4*)(ex + kh * 2048);
      const f32x4 a1 = (kh ? acc[3] : acc[1]) + *(LAS f32x4*)(ex + kh * 2048 + 1024);
      const f32x4 a4 = acc[4] + *(LAS f32x4*)(ex + 4096);
      resP = v4u{pack2(bf2f(resP.x) + a0[0], bfhi(resP.x) + a0[1]), pack2(bf2f(resP.y) + a0[2], bfhi(resP.y) + a0[3]),
                 pack2(bf2f(resP.z) + a1[0], bfhi(resP.z) + a1[1]), pack2(bf2f(resP.w) + a1[2], bfhi(resP.w) + a1[3])};
      resS = v2u{pack2(bf2f(resS.x) + a4[0], bfhi(resS.x) + a4[1]), pack2(bf2f(resS.y) + a4[2], bfhi(resS.y) + a4[3])};
      const u32 x1o = OFF_X1 + it * AT20;
      st128(resP, rs, lane16, x1o + xoffP);
      __builtin_amdgcn_raw_buffer_store_b64(resS, rs, kh ? 0x80000000u : vS, x1o + xoffS, SC1);
    }
    SCHED();
#pragma unroll
    for (int i = E3A; i < E3B; ++i) W[i] = LDW(wi0, i, 0);
    SCHED();
    WAITV(E3B - E3A);
    BAR();
    if (tid == 0) signal(cnt, C_X1 + it * 32 + p);
    TS(8);
    if (w == 0) wait_count(cnt, C_X1 + it * 32 + p, 8);
    BAR();
    TS(9);
    REFRESH();
#pragma unroll
    for (int i = 0; i < 10; ++i) {
      const int ks = 2 * i + (w >> 2), rt = w & 3;
      dma16<true>(srd, lane16, OFF_X1 + it * AT20 + ((4 * p + rt) * 20 + ks) * 1024, lds0 + L_XT + (rt * 20 + ks) * 1024);
    }
    SCHED();
#pragma unroll
    for (int i = E3B; i < 40; ++i) W[i] = LDW(wi0, i, 0);
    const v4u ci0u = __builtin_amdgcn_raw_buffer_load_b128(rs, q * 16, OFF_CI + (m * 16 + 2 * w) * 64, 0);
    const v4u ci1u = __builtin_amdgcn_raw_buffer_load_b128(rs, q * 16 + 64, OFF_CI + (m * 16 + 2 * w) * 64, 0);
    SCHED();
#define wsp0 (OFF_WSP + (p * 8 + w) * 32 * 1024)
    {
      f32x4 acc[4];
#pragma unroll
      for (int rt = 0; rt < 4; ++rt) acc[rt] = f32x4{0.f, 0.f, 0.f, 0.f};
#define P3_STAGE(k0, k1) if (w < 7) { _Pragma("unroll") for (int ks = k0; ks < k1; ++ks) { _Pragma("unroll") for (int rt = 0; rt < 4; ++rt) \
        acc[rt] = mfma(W[ks], *(LAS v4u*)(L + L_XT + (rt * 20 + ks) * 1024 + lane16), acc[rt]); } }
      WAITV(42 - E3B + 8); BAR(); TS(10);
      P3_STAGE(0, 4)
      WAITV(42 - E3B + 5); BAR();
      P3_STAGE(4, 10)
      WAITV(42 - E3B); BAR();
      if (w >= 4) ln_stats(L, w - 4, lane);
      P3_STAGE(10, 20)
      BAR();
      TS(11);
    REFRESH();
      const f32x4 cs = __builtin_bit_cast(f32x4, ci0u);
      float z[4][4], ps[4], pq[4];
      f32x2 st[4];
#pragma unroll
      for (int rt = 0; rt < 4; ++rt) {
        st[rt] = *(LAS f32x2*)(L + L_STAT + (rt * 16 + t) * 8);
        float g[4];
#pragma unroll
        for (int i = 0; i < 4; ++i) g[i] = gelu(st[rt].y * __builtin_fmaf(-st[rt].x, cs[i], acc[rt][i]));
        const u32 p0 = pack2(g[0], g[1]), p1 = pack2(g[2], g[3]);
        z[rt][0] = bf2f(p0); z[rt][1] = bfhi(p0); z[rt][2] = bf2f(p1); z[rt][3] = bfhi(p1);
        ps[rt] = (z[rt][0] + z[rt][1]) + (z[rt][2] + z[rt][3]);
        pq[rt] = __builtin_fmaf(z[rt][0], z[rt][0], z[rt][1] * z[rt][1]) + __builtin_fmaf(z[rt][2], z[rt][2], z[rt][3] * z[rt][3]);
      }
      {
        float s2[2], q2[2];
#pragma unroll
        for (int i = 0; i < 2; ++i) {
          auto r0 = __builtin_amdgcn_permlane32_swap(ubits(ps[i]), ubits(ps[2 + i]), false, false);
          s2[i] = fbits(r0[0]) + fbits(r0[1]);
          auto r1 = __builtin_amdgcn_permlane32_swap(ubits(pq[i]), ubits(pq[2 + i]), false, false);
          q2[i] = fbits(r1[0]) + fbits(r1[1]);
        }
        auto r0 = __builtin_amdgcn_permlane16_swap(ubits(s2[0]), ubits(s2[1]), false, false);
        auto r1 = __builtin_amdgcn_permlane16_swap(ubits(q2[0]), ubits(q2[1]), false, false);
        *(LAS f32x2*)(L + L_PART + w * 512 + lane * 8) = f32x2{fbits(r0[0]) + fbits(r0[1]), fbits(r1[0]) + fbits(r1[1])};
      }
      BAR();
      TS(12);
    REFRESH();
      f32x4 acc1[4];
#pragma unroll
      for (int rt = 0; rt < 4; ++rt) acc1[rt] = f32x4{0.f, 0.f, 0.f, 0.f};
      if (w == 7) {
        f32x2 sm = {0.f, 0.f};
#pragma unroll
        for (int i = 0; i < 7; ++i) sm += *(LAS f32x2*)(L + L_PART + i * 512 + lane * 8);
        const u32 sto = OFF_ST + ((it * 32 + p) * 8) * 1024;
        st128(v4u{ubits(sm.x), 1u, ubits(sm.y), 1u}, rs, lane16, sto + m * 1024);
        u32 spins = 0;
        float ts, tq;
#pragma unroll 1
        while (true) {
          v4u g[8];
#pragma unroll
          for (int i = 0; i < 8; ++i) g[i] = LDW(sto, i, SC1);
          u32 ok = 1u;
          ts = 0.f; tq = 0.f;
#pragma unroll
          for (int i = 0; i < 8; ++i) { ok &= g[i].y & g[i].w; ts += fbits(g[i].x); tq += fbits(g[i].z); }
          if (__all(ok == 1u)) break;
          if (++spins > SPIN_CAP) {
            __hip_atomic_store(cnt + C_ERR * 32, 2u, __ATOMIC_RELAXED, __HIP_MEMORY_SCOPE_AGENT);
            break;
          }
          asm volatile("" ::: "memory");
        }
        const float mean = ts * (1.0f / FH);
        const float var = __builtin_fmaxf(tq * (1.0f / FH) - mean * mean, 0.f);
        *(LAS f32x2*)(L + L_FIN + lane * 8) = f32x2{mean, __builtin_amdgcn_rsqf(var + 1e-5f)};
#pragma unroll
        for (int i = 0; i < E4A; ++i) W[i] = LDW(wsp0, i, 0);
      } else {
#pragma unroll
        for (int i = 0; i < E4A; ++i) W[i] = LDW(wsp0, i, 0);
        SCHED();
#pragma unroll
        for (int ks = 0; ks < 20; ++ks)
#pragma unroll
          for (int rt = 0; rt < 4; ++rt) acc1[rt] = mfma(W[20 + ks], *(LAS v4u*)(L + L_XT + (rt * 20 + ks) * 1024 + lane16), acc1[rt]);
      }
      BAR();
      TS(13);
    REFRESH();
      LAS char* tr = L + L_TR + w * 2048;
#pragma unroll
      for (int rt = 0; rt < 4; ++rt) {
        const f32x2 fs = *(LAS f32x2*)(L + L_FIN + (rt * 16 + t) * 8);
        *(LAS v2u*)(tr + (rt * 16 + t) * 32 + q * 8) =
            v2u{pack2((z[rt][0] - fs.x) * fs.y, (z[rt][1] - fs.x) * fs.y), pack2((z[rt][2] - fs.x) * fs.y, (z[rt][3] - fs.x) * fs.y)};
      }
      asm volatile("s_waitcnt lgkmcnt(0)" ::: "memory");
      const u32 zo = OFF_Z2 + it * ZSZ + ((m * 7 + w) * 64 + 2 * p) * 1024;
#pragma unroll
      for (int k2 = 0; k2 < 2; ++k2) {
        LAS char* src = tr + (32 * k2 + 8 * q + (t >> 2)) * 32 + (t & 3) * 8;
        const v2u lo = __builtin_bit_cast(v2u, __builtin_amdgcn_ds_read_tr16_b64_v4i16((LAS s16x4*)src));
        const v2u hi = __builtin_bit_cast(v2u, __builtin_amdgcn_ds_read_tr16_b64_v4i16((LAS s16x4*)(src + 128)));
        st128(v4u{lo.x, lo.y, hi.x, hi.y}, rs, lane16 + k2 * 1024, (w < nct) ? zo : 0x80000000u);
      }
      SCHED();
#pragma unroll
      for (int i = E4A; i < 32; ++i) W[i] = LDW(wsp0, i, 0);
      SCHED();
      WAITV(32 - E4A);
      BAR();
      if (tid == 0) signal(cnt, C_Z + it * 64 + m * 8 + (p >> 2));
      TS(14);
    REFRESH();
      const f32x4 cs1 = __builtin_bit_cast(f32x4, ci1u);
      if (w < 7) {
#pragma unroll
        for (int rt = 0; rt < 4; ++rt) {
          float g[4];
#pragma unroll
          for (int i = 0; i < 4; ++i) g[i] = gelu(st[rt].y * __builtin_fmaf(-st[rt].x, cs1[i], acc1[rt][i]));
          *(LAS v2u*)(L + L_Z1 + (w * 4 + rt) * 512 + lane * 8) = v2u{pack2(g[0], g[1]), pack2(g[2], g[3])};
        }
      }
    }
    const float bias = bf2f(a.bsp[64 * p + 16 * r + t]);
    wait_count(cnt, C_Z + it * 64 + m * 8 + w, 4);
    TS(15);
    REFRESH();
    {
      const u32 zi = OFF_Z2 + it * ZSZ + (m * 7 * 64 + 8 * w) * 1024;
      v4u Z[RING];
#pragma unroll
      for (int f = 0; f < RING; ++f) Z[f] = LDW(zi + (f >> 3) * 65536, f & 7, SC1);
      SCHED();
      v2u keep = {0u, 0u};
      const u32 uo = OFF_U + it * AT27 + (4 * p + r) * 27 * 1024;
#pragma unroll
      for (int c = 0; c < 7; ++c) {
        if (c < 6 || nct == 7) {
          f32x4 acc[4];
#pragma unroll
          for (int rt = 0; rt < 4; ++rt) acc[rt] = f32x4{0.f, 0.f, 0.f, 0.f};
#pragma unroll
          for (int ks = 0; ks < 8; ++ks) {
            const int f = c * 8 + ks;
            const v4u z = Z[f % RING];
#pragma unroll
            for (int rt = 0; rt < 4; ++rt) acc[rt] = mfma(z, W[ks * 4 + rt], acc[rt]);
            if (f + RING < 56) Z[f % RING] = LDW(zi + ((f + RING) >> 3) * 65536, (f + RING) & 7, SC1);
            SCHED();
          }
          LAS char* red = L + (c & 1) * 32768 + lane16;
#pragma unroll
          for (int rt = 0; rt < 4; ++rt) *(LAS f32x4*)(red + (w * 4 + rt) * 1024) = acc[rt];
          BAR();
          if (kh == ((c >> 1) & 1)) {
            f32x4 sum = *(LAS f32x4*)(red + r * 1024);
#pragma unroll
            for (int i = 1; i < 8; ++i) sum += *(LAS f32x4*)(red + (i * 4 + r) * 1024);
            const v2u z1 = *(LAS v2u*)(L + L_Z1 + (c * 4 + r) * 512 + lane * 8);
            const v2u u = {pack2(bf2f(z1.x) * (sum[0] + bias), bfhi(z1.x) * (sum[1] + bias)),
                           pack2(bf2f(z1.y) * (sum[2] + bias), bfhi(z1.y) * (sum[3] + bias))};
            if (c == 6)
              __builtin_amdgcn_raw_buffer_store_b64(u, rs, vS, uo + (f_single(m) >> 5) * 1024 + ((f_single(m) & 31) >> 3) * 256, SC1);
            else if (c & 1)
              st128(v4u{keep.x, keep.y, u.x, u.y}, rs, lane16, uo + (f_pair(m, c >> 1) >> 5) * 1024);
            else
              keep = u;
          }
        }
      }
    }
    TS(16);
    REFRESH();
    WAITV(0);
    BAR();
    if (tid == 0) signal(cnt, C_U + it * 32 + p);
#pragma unroll
    for (int i = 0; i < 17; ++i) {
      int n = w + 8 * i;
      n = n > 134 ? 134 : n;
      dma16<false>(srd, lane16, OFF_WOUT + (m * 135 + n) * 1024, lds0 + L_XT + n * 1024);
    }
    TS(17);
    if (w == 0) wait_count(cnt, C_U + it * 32 + p, 8);
    WAITV(0);
    BAR();
    TS(18);
    REFRESH();
    {
      v4u A[14];
      const u32 ao = OFF_U + it * AT27 + ((4 * p + r) * 27 + 14 * kh) * 1024;
#pragma unroll
      for (int i = 0; i < 13; ++i) A[i] = LDW(ao, i, SC1);
      A[13] = __builtin_amdgcn_raw_buffer_load_b128(rs, kh ? 0x80000000u : lane16, ao + 13 * 1024, SC1);
      SCHED();
      f32x4 acc[5];
#pragma unroll
      for (int c = 0; c < 5; ++c) acc[c] = f32x4{0.f, 0.f, 0.f, 0.f};
      LAS char* wb = L + L_XT + kh * 14 * 1024 + lane16;
#pragma unroll
      for (int i = 0; i < 13; ++i)
#pragma unroll
        for (int c = 0; c < 5; ++c) acc[c] = mfma(*(LAS v4u*)(wb + (c * 27 + i) * 1024), A[i], acc[c]);
      if (kh == 0) {
#pragma unroll
        for (int c = 0; c < 5; ++c) acc[c] = mfma(*(LAS v4u*)(wb + (c * 27 + 13) * 1024), A[13], acc[c]);
      }
      LAS char* ex = L + L_EXCH + r * 5 * 1024 + lane16;
      if (kh) {
        *(LAS f32x4*)(ex) = acc[0]; *(LAS f32x4*)(ex + 1024) = acc[1]; *(LAS f32x4*)(ex + 4096) = acc[4];
      } else {
        *(LAS f32x4*)(ex + 2048) = acc[2]; *(LAS f32x4*)(ex + 3072) = acc[3];
      }
      BAR();
      TS(19);
    REFRESH();
      const f32x4 a0 = (kh ? acc[2] : acc[0]) + *(LAS f32x4*)(ex + kh * 2048);
      const f32x4 a1 = (kh ? acc[3] : acc[1]) + *(LAS f32x4*)(ex + kh * 2048 + 1024);
      const f32x4 a4 = acc[4] + *(LAS f32x4*)(ex + 4096);
      const v4u oP = {pack2(bf2f(resP.x) + a0[0], bfhi(resP.x) + a0[1]), pack2(bf2f(resP.y) + a0[2], bfhi(resP.y) + a0[3]),
                      pack2(bf2f(resP.z) + a1[0], bfhi(resP.z) + a1[1]), pack2(bf2f(resP.w) + a1[2], bfhi(resP.w) + a1[3])};
      const v2u oS = {pack2(bf2f(resS.x) + a4[0], bfhi(resS.x) + a4[1]), pack2(bf2f(resS.y) + a4[2], bfhi(resS.y) + a4[3])};
      if (it == 0) {
        st128(oP, rs, lane16, OFF_X + XSZ + xoffP);
        __builtin_amdgcn_raw_buffer_store_b64(oS, rs, kh ? 0x80000000u : vS, OFF_X + XSZ + xoffS, SC1);
        SCHED();
#pragma unroll
        for (int i = 0; i < E1; ++i) W[i] = (i & 1) ? LDW(wq1, i >> 1, 0) : LDW(wq0, i >> 1, 0);
        SCHED();
        WAITV(E1);
        BAR();
        if (tid == 0) signal(cnt, C_X2 + p);
      } else {
        const u32 row = (64 * p + 16 * r + t) * (D * 2);
        __builtin_amdgcn_raw_buffer_store_b128(oP, rs_out, row + (d_pair(m, kh) + 8 * q) * 2, 0, 0);
        __builtin_amdgcn_raw_buffer_store_b64(oS, rs_out, kh ? 0x80000000u : row + (d_single(m) + 4 * q) * 2, 0, 0);
      }
    }
    TS(20);
  }
}

}

extern "C" void kernel_launch(void* const* d_in, const int* in_sizes, int n_in, void* d_out, int out_size, void* d_ws,
                              size_t ws_size, hipStream_t stream) {
  PrepArgs pa;
  pa.x = (const u16*)d_in[0];
  pa.wqkv = (const u16*)d_in[1];
  pa.wo = (const u16*)d_in[2];
  pa.win = (const u16*)d_in[4];
  pa.wsp = (const u16*)d_in[5];
  pa.wout = (const u16*)d_in[7];
  pa.ws = (char*)d_ws;
  prep_kernel<<<PB_END, 256, 0, stream>>>(pa);
  MegaArgs ma;
  ma.ws = (char*)d_ws;
  ma.bsp = (const u16*)d_in[6];
  ma.out = (u16*)d_out;
  (void)hipFuncSetAttribute((const void*)mega_kernel, hipFuncAttributeMaxDynamicSharedMemorySize, LDS_BYTES);
  mega_kernel<<<256, 512, LDS_BYTES, stream>>>(ma);
}
```

```cpp
#include <hip/hip_runtime.h>
#include <stdint.h>
#include <utility>

typedef unsigned short u16;
typedef unsigned int u32;
typedef unsigned long long u64;
typedef u32 v4u __attribute__((ext_vector_type(4)));
typedef u32 v2u __attribute__((ext_vector_type(2)));
typedef float f32x4 __attribute__((ext_vector_type(4)));
typedef float f32x2 __attribute__((ext_vector_type(2)));
typedef __bf16 bf16x8 __attribute__((ext_vector_type(8)));
typedef __bf16 bf16x2 __attribute__((ext_vector_type(2)));
typedef short s16x4 __attribute__((ext_vector_type(4)));
#define GAS __attribute__((address_space(1)))
#define LAS __attribute__((address_space(3)))

#define EXP 0
#define STAMPS 0

namespace {

constexpr int S = 2048, D = 640, FH = 864;
constexpr u32 OFF_CNT = 0;
constexpr u32 OFF_ST = 65536;
constexpr u32 ZERO_BYTES = OFF_ST + 2 * 32 * 8 * 64 * 16;
constexpr u32 OFF_CQ = ZERO_BYTES;
constexpr u32 OFF_CI = OFF_CQ + 8192;
constexpr u32 OFF_WQ = OFF_CI + 8192;
constexpr u32 OFF_WO = OFF_WQ + 8 * 16 * 20 * 1024;
constexpr u32 OFF_WI = OFF_WO + 8 * 5 * 20 * 1024;
constexpr u32 OFF_WOUT = OFF_WI + 8 * 16 * 20 * 1024;
constexpr u32 OFF_WSP = OFF_WOUT + 8 * 5 * 27 * 1024;
constexpr u32 AT20 = 128 * 20 * 1024, AT27 = 128 * 27 * 1024, ZSZ = 8 * 7 * 64 * 1024;
constexpr u32 XSZ = AT20 + 20 * 1024;
constexpr u32 OFF_X = OFF_WSP + 32 * 8 * 32 * 1024 + 20 * 1024;
constexpr u32 OFF_O = OFF_X + XSZ + AT20;
constexpr u32 OFF_X1 = OFF_O + 2 * AT20;
constexpr u32 OFF_Z2 = OFF_X1 + 2 * AT20;
constexpr u32 OFF_U = OFF_Z2 + 2 * ZSZ;
constexpr u32 SXSZ = 129 * 2048;
constexpr u32 OFF_SX = OFF_U + 2 * AT27 + 2048;
constexpr u32 OFF_DBG = OFF_SX + 4 * SXSZ;
constexpr u32 WS_BYTES = OFF_DBG + 256 * 256 * 8;

constexpr u32 L_XT = 0;
constexpr u32 L_QKV = 102400;
constexpr u32 QKV_STRIDE = 488;
constexpr u32 L_EXCH = 138240;
constexpr u32 L_PART = 138240;
constexpr u32 L_FIN = 142336;
constexpr u32 L_TR = 143360;
constexpr u32 L_Z1 = 143360;
constexpr u32 L_SX = 143360;
constexpr u32 L_STAT = 158720;
constexpr u32 L_BIAS = 159360;
constexpr u32 L_TS = 159744;
constexpr u32 LDS_BYTES = 160 * 1024;

constexpr int SC1 = 16;
constexpr u32 SPIN_CAP = 1u << 16;

__device__ __forceinline__ float bf2f(u32 lo16) { return __builtin_bit_cast(float, lo16 << 16); }
__device__ __forceinline__ float bfhi(u32 x) { return __builtin_bit_cast(float, x & 0xffff0000u); }
__device__ __forceinline__ u32 pack2(float a, float b) {
  f32x2 v = {a, b};
  return __builtin_bit_cast(u32, __builtin_convertvector(v, bf16x2));
}
__device__ __forceinline__ f32x4 mfma(v4u a, v4u b, f32x4 c) {
  f32x4 d = __builtin_amdgcn_mfma_f32_16x16x32_bf16(__builtin_bit_cast(bf16x8, a), __builtin_bit_cast(bf16x8, b), c, 0, 0, 0);
  asm volatile("" : "+v"(d));
  return d;
}
__device__ __forceinline__ float dot2(u32 a, u32 b, float c) {
  return __builtin_amdgcn_fdot2_f32_bf16(__builtin_bit_cast(bf16x2, a), __builtin_bit_cast(bf16x2, b), c, false);
}
__device__ __forceinline__ float fbits(u32 x) { return __builtin_bit_cast(float, x); }
__device__ __forceinline__ u32 ubits(float x) { return __builtin_bit_cast(u32, x); }
__device__ __forceinline__ float xsum32(float x) {
  auto r = __builtin_amdgcn_permlane32_swap(ubits(x), ubits(x), false, false);
  return fbits(r[0]) + fbits(r[1]);
}
__device__ __forceinline__ float xsum16(float x) {
  auto r = __builtin_amdgcn_permlane16_swap(ubits(x), ubits(x), false, false);
  return fbits(r[0]) + fbits(r[1]);
}
__device__ __forceinline__ float gelu(float x) {
  const float t = __builtin_fmaf(x * x, -0.10294324f, -2.30220820f);
  const float e = __builtin_amdgcn_exp2f(x * t);
  return x * __builtin_amdgcn_rcpf(1.0f + e);
}

__device__ __forceinline__ void st128(v4u v, __amdgpu_buffer_rsrc_t rs, u32 voff, u32 soff) {
  __builtin_amdgcn_raw_buffer_store_b128(v, rs, voff + soff, 0, SC1);
}
__device__ __forceinline__ float add1(float x, float y) { float z; asm("v_add_f32 %0, %1, %2" : "=v"(z) : "v"(x), "v"(y)); return z; }
__device__ __forceinline__ float mul1(float x, float y) { float z; asm("v_mul_f32 %0, %1, %2" : "=v"(z) : "v"(x), "v"(y)); return z; }
__device__ __forceinline__ void keepalive(f32x4 v) { asm volatile("" :: "v"(v)); }
#define WAITV(n) asm volatile("s_waitcnt vmcnt(%0)" ::"n"(n) : "memory")
#define BAR() asm volatile("s_waitcnt lgkmcnt(0)\n\ts_barrier" ::: "memory")
#define BARV(n) asm volatile("s_waitcnt vmcnt(%0)\n\ts_barrier" ::"n"(n) : "memory")
#define BARVL(n) asm volatile("s_waitcnt vmcnt(%0) lgkmcnt(0)\n\ts_barrier" ::"n"(n) : "memory")
#define SCHED() __builtin_amdgcn_sched_barrier(0)

template <bool COH, int IMM>
__device__ __forceinline__ void dma16(v4u srd, u32 voff, u32 soff, u32 ldsoff) {
  if (COH)
    asm volatile("s_mov_b32 m0, %0\n\ts_nop 0\n\tbuffer_load_dwordx4 %1, %2, %3 offen offset:%4 sc1 lds" ::"s"(ldsoff), "v"(voff), "s"(srd), "s"(soff), "n"(IMM) : "memory");
  else
    asm volatile("s_mov_b32 m0, %0\n\ts_nop 0\n\tbuffer_load_dwordx4 %1, %2, %3 offen offset:%4 lds" ::"s"(ldsoff), "v"(voff), "s"(srd), "s"(soff), "n"(IMM) : "memory");
}

__host__ __device__ constexpr int d_pair(int m, int P) { return 80 * m + ((m & 1) ? 16 : 0) + 32 * P; }
__host__ __device__ constexpr int d_single(int m) { return 80 * m + ((m & 1) ? 0 : 64); }
__host__ __device__ constexpr int f_base(int m) { return m < 7 ? 112 * m : 768; }
__host__ __device__ constexpr int f_pair(int m, int P) { return f_base(m) + (((m & 1) && m < 6) ? 16 : 0) + 32 * P; }
__host__ __device__ constexpr int f_single(int m) { return f_base(m) + ((m & 1) ? 0 : 96); }

struct PrepArgs {
  const u16 *x, *wqkv, *wo, *win, *wsp, *wout;
  char* ws;
};

__device__ __forceinline__ int qkv_col(int m, int slot, int s) {
  const int w = slot >> 1, j = slot & 1;
  int kv = -1, q = -1;
  if (j == 0) kv = w;
  else if (w == 2) kv = 8;
  else if (w == 3) kv = 9;
  else if (w == 7) kv = 7;
  else q = (w < 2) ? w : w - 2;
  if (q >= 0) return 80 * m + 16 * q + s;
  return (kv < 5) ? 640 + 80 * m + 16 * kv + s : 1280 + 80 * m + 16 * (kv - 5) + s;
}
__device__ __forceinline__ int dout_col(int m, int ct, int s) {
  if (ct < 4) return d_pair(m, ct >> 1) + 8 * (s >> 2) + 4 * (ct & 1) + (s & 3);
  return d_single(m) + s;
}
__device__ __forceinline__ int f_col(int m, int ct, int s) {
  if (ct < 6) return f_pair(m, ct >> 1) + 8 * (s >> 2) + 4 * (ct & 1) + (s & 3);
  return (m < 6) ? f_single(m) + s : -1;
}
__device__ __forceinline__ int o_row(int k) {
  const int id = k >> 3, j = k & 7, m = id / 10, c = id % 10;
  if (c < 8) return 80 * m + 20 * (c >> 1) + 8 * (c & 1) + j;
  return 80 * m + 20 * (2 * (c - 8) + (j >> 2)) + 16 + (j & 3);
}

constexpr int PB_WQ = 0, PB_WI = 128, PB_WO = 256, PB_WOUT = 296, PB_WSP = 336, PB_X = 592, PB_ZERO = 624, PB_ZX = 660, PB_END = 662;

__global__ void __launch_bounds__(256) prep_kernel(PrepArgs a) {
  __shared__ float red[4][16];
  const int b = blockIdx.x, tid = threadIdx.x, lane = tid & 63, wv = tid >> 6;
  const int s = lane & 15, kq = lane >> 4;
  if (b < PB_WSP) {
    const u16* src;
    int ld, col, nks, kind;
    u32 dst;
    float* cs = nullptr;
    if (b < PB_WI) {
      const int m = b >> 4, slot = b & 15;
      src = a.wqkv; ld = 1920; col = qkv_col(m, slot, s); nks = 20; kind = 0;
      dst = OFF_WQ + b * 20 * 1024; cs = (float*)(a.ws + OFF_CQ) + b * 16;
    } else if (b < PB_WO) {
      const int bb = b - PB_WI, m = bb >> 4, slot = bb & 15, ct = slot >> 1;
      src = a.win; ld = 1728; nks = 20; kind = 0;
      col = (ct < 7) ? f_col(m, ct, s) : -1;
      if (col >= 0 && !(slot & 1)) col += FH;
      dst = OFF_WI + bb * 20 * 1024; cs = (float*)(a.ws + OFF_CI) + bb * 16;
    } else if (b < PB_WOUT) {
      const int bb = b - PB_WO;
      src = a.wo; ld = 640; col = dout_col(bb / 5, bb % 5, s); nks = 20; kind = 1;
      dst = OFF_WO + bb * 20 * 1024;
    } else {
      const int bb = b - PB_WOUT;
      src = a.wout; ld = 640; col = dout_col(bb / 5, bb % 5, s); nks = 27; kind = 0;
      dst = OFF_WOUT + bb * 27 * 1024;
    }
    float sum = 0.f;
#pragma unroll
    for (int i = 0; i < 7; ++i) {
      const int ks = wv + 4 * i;
      if (ks < nks) {
        u32 e[8];
#pragma unroll
        for (int j = 0; j < 8; ++j) {
          int k = 32 * ks + 8 * kq + j;
          if (kind == 1) k = o_row(k);
          e[j] = (col >= 0) ? (u32)src[(size_t)k * ld + col] : 0u;
        }
#pragma unroll
        for (int j = 0; j < 8; ++j) sum += bf2f(e[j]);
        v4u v = {e[0] | (e[1] << 16), e[2] | (e[3] << 16), e[4] | (e[5] << 16), e[6] | (e[7] << 16)};
        *(v4u*)(a.ws + dst + ks * 1024 + lane * 16) = v;
      }
    }
    if (cs) {
      sum = xsum32(xsum16(sum));
      if (lane < 16) red[wv][lane] = sum;
      __syncthreads();
      if (tid < 16) cs[tid] = (red[0][tid] + red[1][tid]) + (red[2][tid] + red[3][tid]);
    }
  } else if (b < PB_X) {
    const int bb = b - PB_WSP, p = bb >> 3, w = bb & 7;
#pragma unroll
    for (int ks = 0; ks < 8; ++ks) {
      const v4u v = *(const v4u*)(a.wsp + (size_t)(64 * p + 16 * wv + s) * S + 256 * w + 32 * ks + 8 * kq);
      *(v4u*)(a.ws + OFF_WSP + (bb * 32 + ks * 4 + wv) * 1024 + lane * 16) = v;
    }
  } else if (b < PB_ZERO) {
    const int rt = (b - PB_X) * 4 + wv;
    float sm = 0.f, sq = 0.f;
#pragma unroll
    for (int ks = 0; ks < 20; ++ks) {
      const v4u v = *(const v4u*)(a.x + (size_t)(16 * rt + s) * D + 32 * ks + 8 * kq);
      *(v4u*)(a.ws + OFF_X + (rt * 20 + ks) * 1024 + lane * 16) = v;
#pragma unroll
      for (int j = 0; j < 4; ++j) { sm = dot2(v[j], 0x3f803f80u, sm); sq = dot2(v[j], v[j], sq); }
    }
    sm = xsum32(xsum16(sm)); sq = xsum32(xsum16(sq));
    float* sx = (float*)(a.ws + OFF_SX + rt * 2048);
#pragma unroll
    for (int i = 0; i < 8; ++i) sx[i * 64 + lane] = (i == 0 && lane < 32) ? (lane < 16 ? sm : sq) : 0.f;
  } else if (b >= PB_ZX) {
#pragma unroll
    for (int i = 0; i < 5; ++i) *(v4u*)(a.ws + OFF_X + (b - PB_ZX) * XSZ - 20 * 1024 + i * 4096 + tid * 16) = v4u{0u, 0u, 0u, 0u};
    if (tid < 128) *(v4u*)(a.ws + OFF_SX + (b - PB_ZX) * 2 * SXSZ - 2048 + tid * 16) = v4u{0u, 0u, 0u, 0u};
  } else {
    const int bb = b - PB_ZERO;
#pragma unroll
    for (int i = 0; i < 4; ++i) *(v4u*)(a.ws + (bb * 4 + i) * 4096 + tid * 16) = v4u{0u, 0u, 0u, 0u};
  }
}
static_assert((PB_ZX - PB_ZERO) * 16384 == ZERO_BYTES, "zero range");

struct MegaArgs {
  char* ws;
  const u16* bsp;
  u16* out;
};

struct Poll { u32 v0, v1, v2; };
__device__ __forceinline__ Poll wait_count(GAS u32* cnt, u32 idx, int n, u32 want, int lane) {
  u32 left = SPIN_CAP;
  GAS u32* ptr = cnt + idx + (lane < n ? lane : 0);
  Poll pl;
  asm volatile(
      "1:\n\t"
      "global_load_dword %0, %4, off sc1\n\ts_waitcnt vmcnt(0)\n\tv_cmp_gt_u32 vcc, %5, %0\n\ts_cbranch_vccz 2f\n\t"
      "s_sub_u32 %3, %3, 1\n\ts_cbranch_scc0 1b\n"
      "2:"
      : "=&v"(pl.v0), "=&v"(pl.v1), "=&v"(pl.v2), "+s"(left)
      : "v"(ptr), "s"(want)
      : "vcc", "scc", "memory");
  return pl;
}
__device__ __forceinline__ void poll_done(const Poll& pl) { asm volatile("" ::"v"(pl.v0), "v"(pl.v1), "v"(pl.v2)); }
__device__ __forceinline__ void signal(GAS u32* cnt, u32 idx, u32 stride, int n, u32 phase, int lane) {
  if (lane < n) __hip_atomic_store(cnt + idx + lane * stride, phase, __ATOMIC_RELAXED, __HIP_MEMORY_SCOPE_AGENT);
}

#define TS(i) do {} while (0)
#define TS7(i) do {} while (0)
#define TSX(i) do {} while (0)

#define LDV(voff, off_s, i, aux) __builtin_amdgcn_raw_buffer_load_b128(rs, (voff) + (((i) & 3) << 10), (off_s) + (((i) >> 2) << 12), (aux))
#define LDW(off_s, i, aux) LDV(lane16, off_s, i, aux)
#define UNROLL _Pragma("unroll")
template <int B, class F, int... I>
__device__ __forceinline__ void sfor_impl(F&& f, std::integer_sequence<int, I...>) { (f(std::integral_constant<int, B + I>{}), ...); }
#define SFOR(i, B, E, ...) sfor_impl<B>([&](auto i##_) __attribute__((always_inline)) { constexpr int i = decltype(i##_)::value; __VA_ARGS__ }, std::make_integer_sequence<int, (E) - (B)>{})
constexpr u32 OOB = 0x80000000u;
constexpr u32 ONES = 0x3f803f80u;
constexpr f32x4 ZERO4 = {0.f, 0.f, 0.f, 0.f};
#define RINGN 6
constexpr int RING = RINGN;

template <bool W7>
__device__ __forceinline__ void mega_body(const MegaArgs& a, LAS char* L, const int w_c) {
  const u32 lds0 = (u32)(uintptr_t)L;
  const int tid_c = threadIdx.x;
  const int b = blockIdx.x;
  const int p_c = (b & 7) * 4 + (b >> 6), m_c = (b >> 3) & 7;
  int tid, w, p, m, lane, q, t, r, kh;
  u32 lane16;
  LAS char *B0, *B1;
#define REFRESH() do { tid = tid_c; w = w_c; p = p_c; m = m_c; asm volatile("" : "+v"(tid)); asm volatile("" : "+s"(w), "+s"(p), "+s"(m)); if (W7) w = 7; lane = tid & 63; lane16 = lane * 16; \
    q = lane >> 4; t = lane & 15; r = w & 3; kh = w >> 2; } while (0)
#define BASES(off) do { u32 b0_ = lds0 + lane16 + (off), b1_ = b0_ + 65536; asm volatile("" : "+v"(b0_), "+v"(b1_)); B0 = (LAS char*)(uintptr_t)b0_; B1 = (LAS char*)(uintptr_t)b1_; } while (0)
#define FRAG(n) ((n) < 64 ? *(LAS v4u*)(B0 + (n) * 1024) : *(LAS v4u*)(B1 + ((n) - 64) * 1024))
  REFRESH();
  const __amdgpu_buffer_rsrc_t rs = __builtin_amdgcn_make_buffer_rsrc((void*)a.ws, 0, (int)WS_BYTES, 0x00020000);
  const __amdgpu_buffer_rsrc_t rs_out = __builtin_amdgcn_make_buffer_rsrc((void*)a.out, 0, S * D * 2, 0x00020000);
  const v4u srd = {(u32)(uintptr_t)a.ws, (u32)((uintptr_t)a.ws >> 32) & 0xffffu, WS_BYTES, 0x00020000u};
  GAS u32* cnt = (GAS u32*)(a.ws + OFF_CNT);
#define wq0 (OFF_WQ + (m * 16 + 2 * w) * 20 * 1024)
#define wq1 (wq0 + 20 * 1024)
#define wi0 (OFF_WI + (m * 16 + 2 * w) * 20 * 1024)
#define wsp0 (OFF_WSP + (p * 8 + w) * 32 * 1024)
#define xoffP (((4 * p + r) * 20 + (d_pair(m, kh) >> 5)) * 1024)
#define xoffS (((4 * p + r) * 20 + (d_single(m) >> 5)) * 1024 + ((d_single(m) & 31) >> 3) * 256)
#define vS ((u32)(((q >> 1) * 16 + t) * 16 + (q & 1) * 8))

#define SX_FETCH(buf, rt0, NRT) do { const u32 g_ = OFF_SX + (buf) * SXSZ + (rt0) * 2048; \
    SFOR(i, 0, 2 * (NRT), { dma16<true, (i & 3) * 1024>(srd, lane16, g_ + (i >> 2) * 4096, lds0 + L_SX + (i >> 2) * 4096); }); } while (0)
#define SX_FINISH(NRT) do { UNROLL for (int h_ = 0; h_ < ((NRT) + 3) / 4; ++h_) { \
      LAS float* sp_ = (LAS float*)(L + L_SX + (h_ ? 4 : q) * 2048 + t * 4); \
      float sm_ = 0.f, sq_ = 0.f; \
      UNROLL for (int i = 0; i < 16; ++i) { sm_ = add1(sm_, sp_[i * 32]); sq_ = add1(sq_, sp_[i * 32 + 16]); } \
      const float mean_ = sm_ * (1.0f / D); \
      const float rstd_ = __builtin_amdgcn_rsqf(__builtin_fmaxf(sq_ * (1.0f / D) - mean_ * mean_, 0.f) + 1e-5f); \
      *(LAS f32x2*)(L + L_STAT + ((h_ ? 4 : q) * 16 + t) * 8) = f32x2{-mean_ * rstd_, rstd_}; } } while (0)
#define SX_PUBLISH(buf) do { \
    float sm_ = dot2(resP.x, ONES, dot2(resP.y, ONES, dot2(resP.z, ONES, dot2(resP.w, ONES, 0.f)))); \
    float sq_ = dot2(resP.x, resP.x, dot2(resP.y, resP.y, dot2(resP.z, resP.z, dot2(resP.w, resP.w, 0.f)))); \
    if (kh == 0) { sm_ = dot2(resS.x, ONES, dot2(resS.y, ONES, sm_)); sq_ = dot2(resS.x, resS.x, dot2(resS.y, resS.y, sq_)); } \
    auto r_ = __builtin_amdgcn_permlane32_swap(ubits(sm_), ubits(sq_), false, false); \
    const float v_ = xsum16(fbits(r_[0]) + fbits(r_[1])); \
    __builtin_amdgcn_raw_buffer_store_b32(ubits(v_), rs, (q & 1) ? OOB : OFF_SX + (buf) * SXSZ + ((4 * p + r) * 16 + 2 * m + kh) * 128 + (q >> 1) * 64 + t * 4, 0, SC1); } while (0)

  Poll pl = {0u, 0u, 0u};
  v4u Q0[20], Q1[20];
  UNROLL for (int i = 0; i < 10; ++i) { Q0[i] = LDW(wq0, i, 0); if (!W7) Q1[i] = LDW(wq1, i, 0); }
  v4u resP = __builtin_amdgcn_raw_buffer_load_b128(rs, lane16, OFF_X + xoffP, 0);
  v2u resS = __builtin_amdgcn_raw_buffer_load_b64(rs, vS, OFF_X + xoffS, 0);
  if (W7) *(LAS float*)(L + L_BIAS + lane * 4) = bf2f(a.bsp[64 * p + lane]);
  u32 o5[5];
  UNROLL for (int i = 0; i < 5; ++i) {
    const int n = w + 8 * i, ks = (n * 205) >> 10;
    o5[i] = ((n - 5 * ks) * 20 + ks) * 1024;
    asm volatile("" : "+s"(o5[i]));
  }
  u32 xg1 = OFF_X + (4 * p - 1) * 20 * 1024;
  SCHED();

#pragma unroll 1
  for (int it = 0; it < 2; ++it) {
    TS(1);
    REFRESH();
    v4u WI[40];
    {
      asm volatile("" : "+s"(xg1));
      const u32 xg = xg1;
      xg1 += XSZ;
#define XDMA(i) do { u32 o_ = o5[(i) % 5] + ((i) / 5) * 8192; if ((i) == 12) o_ = w < 4 ? o_ : o5[1] + 2 * 8192; \
        dma16<true, 0>(srd, lane16, xg + o_, lds0 + L_XT + o_); } while (0)
      UNROLL for (int i = 0; i < 5; ++i) XDMA(i);
      BASES(L_XT);
      f32x4 acc[2][5];
      v4u cs0u, cs1u;
      BARV(3);
      if (W7) poll_done(pl);
      TS(2);
      const bool halo1 = w == 2 || w == 3;
      {
        v4u x[100];
        UNROLL for (int i = 0; i < 4; ++i) x[i] = FRAG(i * 20);
        if (!W7) acc[1][0] = ZERO4;
        SFOR(i, 0, 100, {
          constexpr int ks = i / 5; constexpr int rt = i % 5;
          if constexpr (i == 11) BARV(6);
          if constexpr (i == 36) BARV(W7 ? 4 : 8);
          acc[0][rt] = mfma(Q0[ks], x[i], ks ? acc[0][rt] : ZERO4);
          if constexpr (i + 4 < 100) x[i + 4] = FRAG(((i + 4) % 5) * 20 + (i + 4) / 5);
          if constexpr (!W7 && rt > 0) acc[1][rt] = mfma(Q1[ks], x[i], ks ? acc[1][rt] : ZERO4);
          if constexpr (!W7 && rt == 0) { if (halo1) acc[1][0] = mfma(Q1[ks], x[i], acc[1][0]); }
          if constexpr (i < 16 && !(i & 1)) XDMA(5 + i / 2);
          if constexpr (W7 && i == 15) SX_FETCH(2 * it, 4 * p - 1, 5);
          if constexpr (W7 && i == 40) SX_FINISH(5);
          if constexpr (ks >= 3 && ks < 13 && rt == 1) Q0[ks + 7] = LDW(wq0, ks + 7, 0);
          if constexpr (!W7 && ks >= 3 && ks < 13 && rt == 3) Q1[ks + 7] = LDW(wq1, ks + 7, 0);
          if constexpr (ks == 13 && rt == 2) cs0u = __builtin_amdgcn_raw_buffer_load_b128(rs, q * 16, OFF_CQ + (m * 16 + 2 * w) * 64, 0);
          if constexpr (!W7 && ks == 13 && rt == 4) cs1u = __builtin_amdgcn_raw_buffer_load_b128(rs, q * 16 + 64, OFF_CQ + (m * 16 + 2 * w) * 64, 0);
          if constexpr (!W7 && ks >= 14 && rt == 1) WI[ks - 14] = LDW(wi0, ks - 14, 0);
          SCHED();
        });
      }
      asm volatile("" : "+v"(cs0u));
      if (!W7) asm volatile("" : "+v"(cs1u));
      SCHED();
      BAR();
      TS(3);
      REFRESH();
      const u32 wog = OFF_WO + (m * 100 + 13 * w) * 1024, wol = lds0 + L_XT + 13 * w * 1024;
#define WODMA(i) do { if (!W7 || (i) < 9) dma16<false, ((i) & 3) * 1024>(srd, lane16, wog + ((i) >> 2) * 4096, wol + ((i) >> 2) * 4096); } while (0)
      const f32x4 cs[2] = {__builtin_bit_cast(f32x4, cs0u), __builtin_bit_cast(f32x4, cs1u)};
      const int col0 = 80 + 16 * w;
      const int col1 = (int)((0xC432ED10u >> (4 * w)) & 15u) * 16;
      f32x2 st[5];
      UNROLL for (int rt = 0; rt < 5; ++rt) st[rt] = *(LAS f32x2*)(L + L_STAT + (rt * 16 + t) * 8);
      LAS char* qw0 = L + L_QKV + t * QKV_STRIDE + (col0 + 4 * q) * 2;
      LAS char* qw1 = L + L_QKV + t * QKV_STRIDE + (col1 + 4 * q) * 2;
      UNROLL for (int rt = 0; rt < 5; ++rt) {
        UNROLL for (int j = 0; j < (W7 ? 1 : 2); ++j) {
          f32x4 v;
          UNROLL for (int i = 0; i < 4; ++i) v[i] = __builtin_fmaf(acc[j][rt][i], st[rt].y, st[rt].x * cs[j][i]);
          *(LAS v2u*)((j ? qw1 : qw0) + rt * 16 * QKV_STRIDE) = v2u{pack2(v[0], v[1]), pack2(v[2], v[3])};
        }
        if (rt == 0) { WODMA(0); WODMA(1); } else if (rt == 1) WODMA(2); else if (rt == 2) WODMA(3); else if (rt == 3) WODMA(4); else WODMA(5);
      }
      BAR();
      TS(4);
      {
        const int h = w & 3, hf = lane >> 5, tok = (w >> 2) * 32 + (lane & 31);
        LAS char* rowq = L + L_QKV + (16 + tok) * QKV_STRIDE + h * 40;
        LAS char* rowk = rowq + 160 - (hf * 8 + 6) * QKV_STRIDE;
        v2u qv[5], kv[4][5], vv[4][5];
        UNROLL for (int c = 0; c < 5; ++c) qv[c] = *(LAS v2u*)(rowq + c * 8);
        UNROLL for (int i = 0; i < 4; ++i) UNROLL for (int c = 0; c < 5; ++c) kv[i][c] = *(LAS v2u*)(rowk + (3 - i) * 2 * QKV_STRIDE + c * 8);
        WODMA(6); WODMA(7);
        UNROLL for (int i = 0; i < 4; ++i) UNROLL for (int c = 0; c < 5; ++c) vv[i][c] = *(LAS v2u*)(rowk + 160 + (3 - i) * 2 * QKV_STRIDE + c * 8);
        WODMA(8);
        float lg[4];
        UNROLL for (int i = 0; i < 4; ++i) {
          float s0 = 0.f, s1 = 0.f;
          UNROLL for (int c = 0; c < 5; ++c) {
            s0 = dot2(qv[c].x, kv[i][c].x, s0);
            s1 = dot2(qv[c].y, kv[i][c].y, s1);
          }
          lg[i] = (s0 + s1) * (0.22360679774997896f * 1.4426950408889634f);
          if (64 * p + tok - 2 * (4 * hf + i) < 0) lg[i] = -1.0e30f;
        }
        SCHED();
        WODMA(9); WODMA(10);
        float mx = __builtin_fmaxf(__builtin_fmaxf(lg[0], lg[1]), __builtin_fmaxf(lg[2], lg[3]));
        {
          auto rr = __builtin_amdgcn_permlane32_swap(ubits(mx), ubits(mx), false, false);
          mx = __builtin_fmaxf(fbits(rr[0]), fbits(rr[1]));
        }
        float pr[4];
        UNROLL for (int i = 0; i < 4; ++i) pr[i] = __builtin_amdgcn_exp2f(lg[i] - mx);
        const float inv = __builtin_amdgcn_rcpf(xsum32((pr[0] + pr[1]) + (pr[2] + pr[3])));
        const u32 P01 = pack2(pr[0] * inv, pr[1] * inv), P23 = pack2(pr[2] * inv, pr[3] * inv);
        SCHED();
        WODMA(11);
        float o[20];
        UNROLL for (int c = 0; c < 5; ++c) {
          o[4 * c + 0] = dot2(__builtin_amdgcn_perm(vv[3][c].x, vv[2][c].x, 0x05040100u), P23, dot2(__builtin_amdgcn_perm(vv[1][c].x, vv[0][c].x, 0x05040100u), P01, 0.f));
          o[4 * c + 1] = dot2(__builtin_amdgcn_perm(vv[3][c].x, vv[2][c].x, 0x07060302u), P23, dot2(__builtin_amdgcn_perm(vv[1][c].x, vv[0][c].x, 0x07060302u), P01, 0.f));
          o[4 * c + 2] = dot2(__builtin_amdgcn_perm(vv[3][c].y, vv[2][c].y, 0x05040100u), P23, dot2(__builtin_amdgcn_perm(vv[1][c].y, vv[0][c].y, 0x05040100u), P01, 0.f));
          o[4 * c + 3] = dot2(__builtin_amdgcn_perm(vv[3][c].y, vv[2][c].y, 0x07060302u), P23, dot2(__builtin_amdgcn_perm(vv[1][c].y, vv[0][c].y, 0x07060302u), P01, 0.f));
          if (c == 0) { SCHED(); WODMA(12); }
        }
        float f[10];
        UNROLL for (int d = 0; d < 8; ++d) {
          auto rr = __builtin_amdgcn_permlane32_swap(ubits(o[d]), ubits(o[8 + d]), false, false);
          f[d] = fbits(rr[0]) + fbits(rr[1]);
        }
        UNROLL for (int d = 0; d < 2; ++d) {
          auto rr = __builtin_amdgcn_permlane32_swap(ubits(o[16 + d]), ubits(o[18 + d]), false, false);
          f[8 + d] = fbits(rr[0]) + fbits(rr[1]);
        }
        const v4u ov = {pack2(f[0], f[1]), pack2(f[2], f[3]), pack2(f[4], f[5]), pack2(f[6], f[7])};
        const u32 lp = pack2(f[8], f[9]);
        auto lr = __builtin_amdgcn_permlane32_swap(lp, lp, false, false);
        const u32 obase = OFF_O + it * AT20 + (4 * p + (tok >> 4)) * 20 * 1024 + (tok & 15) * 16;
        const int id = 10 * m + 2 * h + hf, id2 = 10 * m + 8 + (h >> 1);
        __builtin_amdgcn_raw_buffer_store_b128(ov, rs, obase + id * 256, 0, SC1);
        __builtin_amdgcn_raw_buffer_store_b64(v2u{lr[0], lr[1]}, rs, hf ? OOB : obase + id2 * 256 + (h & 1) * 8, 0, SC1);
      }
      SCHED();
      if (!W7) { UNROLL for (int i = 6; i < 10; ++i) WI[i] = LDW(wi0, i, 0); }
      SCHED();
      TS(5); TS7(0);
      BARV(W7 ? 0 : 4);
    }
    TS(6);
    REFRESH();
    {
      BASES(L_XT + kh * 10 * 1024);
      v4u Bf[50];
#define WOF(i) FRAG(((i) % 5) * 20 + (i) / 5)
      if constexpr (W7) {
        signal(cnt, p * 512 + m, 64, 8, 4 * it + 1, lane);
        pl = wait_count(cnt, (p * 8 + m) * 64, 8, 4 * it + 1, lane);
        TS7(1);
      } else {
        UNROLL for (int i = 10; i < 18; ++i) WI[i] = LDW(wi0, i, 0);
      }
      UNROLL for (int i = 0; i < 8; ++i) Bf[i] = WOF(i);
      u32 ao = OFF_O + it * AT20 + ((4 * p + r) * 20 + 10 * kh) * 1024;
      asm volatile("" : "+s"(ao));
      SCHED();
      asm volatile("s_barrier" ::: "memory");
      TS(7);
      v4u A[10];
      UNROLL for (int i = 0; i < 5; ++i) A[i] = LDW(ao, i, SC1);
      SCHED();
      f32x4 acc[5];
      SFOR(i, 0, 50, {
        acc[i % 5] = mfma(Bf[i], A[i / 5], i < 5 ? ZERO4 : acc[i % 5]);
        if constexpr (W7 && i == 1) poll_done(pl);
        if constexpr (i + 8 < 50) Bf[i + 8] = WOF(i + 8);
        if constexpr (i % 5 == 0 && i < 25) A[i / 5 + 5] = LDW(ao, i / 5 + 5, SC1);
        if constexpr (!W7 && i % 5 == 0 && i >= 25 && i < 40) WI[18 + (i - 25) / 5] = LDW(wi0, 18 + (i - 25) / 5, 0);
        SCHED();
      });
      LAS char* ex = L + L_EXCH + r * 5 * 1024 + lane16;
      if (kh) {
        *(LAS f32x4*)(ex) = acc[0]; *(LAS f32x4*)(ex + 1024) = acc[1]; *(LAS f32x4*)(ex + 4096) = acc[4];
      } else {
        *(LAS f32x4*)(ex + 2048) = acc[2]; *(LAS f32x4*)(ex + 3072) = acc[3];
      }
      const u32 xa = lane16 + OFF_X1 + it * AT20 + xoffP, xb = kh ? OOB : vS + OFF_X1 + it * AT20 + xoffS;
      BAR();
      TS(8);
      const f32x4 a0 = (kh ? acc[2] : acc[0]) + *(LAS f32x4*)(ex + kh * 2048);
      const f32x4 a1 = (kh ? acc[3] : acc[1]) + *(LAS f32x4*)(ex + kh * 2048 + 1024);
      const f32x4 a4 = acc[4] + *(LAS f32x4*)(ex + 4096);
      resP = v4u{pack2(bf2f(resP.x) + a0[0], bfhi(resP.x) + a0[1]), pack2(bf2f(resP.y) + a0[2], bfhi(resP.y) + a0[3]),
                 pack2(bf2f(resP.z) + a1[0], bfhi(resP.z) + a1[1]), pack2(bf2f(resP.w) + a1[2], bfhi(resP.w) + a1[3])};
      resS = v2u{pack2(bf2f(resS.x) + a4[0], bfhi(resS.x) + a4[1]), pack2(bf2f(resS.y) + a4[2], bfhi(resS.y) + a4[3])};
      __builtin_amdgcn_raw_buffer_store_b128(resP, rs, xa, 0, SC1);
      __builtin_amdgcn_raw_buffer_store_b64(resS, rs, xb, 0, SC1);
      SX_PUBLISH(2 * it + 1);
      SCHED();
      if (!W7) { UNROLL for (int i = 21; i < 25; ++i) WI[i] = LDW(wi0, i, 0); }
      SCHED();
      BARV(W7 ? 0 : 4);
    }
    TS(9);
    REFRESH();
    v4u WS[32];
    {
      u32 xl = (r * 20 + kh) * 1024, xg = OFF_X1 + it * AT20 + 4 * p * 20 * 1024 + xl;
      xl += lds0 + L_XT;
      asm volatile("" : "+s"(xl), "+s"(xg));
#define X1DMA(i) dma16<true, ((i) & 1) * 2048>(srd, lane16, xg + ((i) >> 1) * 4096, xl + ((i) >> 1) * 4096)
#define X1F(i) FRAG(((i) & 3) * 20 + ((i) >> 2))
      if constexpr (W7) {
        signal(cnt, p * 512 + m, 64, 8, 4 * it + 2, lane);
        pl = wait_count(cnt, (p * 8 + m) * 64, 8, 4 * it + 2, lane);
        TS7(2);
        asm volatile("s_barrier" ::: "memory");
        X1DMA(0); X1DMA(1); X1DMA(2); X1DMA(3); X1DMA(4);
        BARV(3);
        poll_done(pl);
        X1DMA(5); X1DMA(6); X1DMA(7);
        BARV(3);
        X1DMA(8); X1DMA(9);
        SX_FETCH(2 * it + 1, 4 * p, 4);
        BARV(8);
        UNROLL for (int i = 0; i < 8; ++i) WS[i] = LDW(wsp0, i, 0);
        SCHED();
        WAITV(8);
        SX_FINISH(4);
        BAR();
        UNROLL for (int i = 8; i < 32; ++i) WS[i] = LDW(wsp0, i, 0);
        u32 sto = OFF_ST + ((it * 32 + p) * 8) * 1024;
        asm volatile("" : "+s"(sto));
        SCHED();
        BAR();
        f32x2 sm = {0.f, 0.f};
        UNROLL for (int i = 0; i < 7; ++i) sm += *(LAS f32x2*)(L + L_PART + i * 512 + lane * 8);
        st128(v4u{ubits(sm.x), 1u, ubits(sm.y), 1u}, rs, lane16, sto + m * 1024);
        WAITV(0);
        TS7(3);
        u32 spins = 0;
        float ts, tq;
#pragma unroll 1
        while (true) {
          v4u g[8];
          UNROLL for (int i = 0; i < 8; ++i) g[i] = LDW(sto, i, SC1);
          u32 ok = 1u;
          ts = 0.f; tq = 0.f;
          UNROLL for (int i = 0; i < 8; ++i) { ok &= g[i].y & g[i].w; ts += fbits(g[i].x); tq += fbits(g[i].z); }
          if (__all(ok == 1u)) break;
          if (++spins > SPIN_CAP) break;
          asm volatile("" ::: "memory");
        }
        const float mean = ts * (1.0f / FH);
        const float var = __builtin_fmaxf(tq * (1.0f / FH) - mean * mean, 0.f);
        const float rstd = __builtin_amdgcn_rsqf(var + 1e-5f);
        *(LAS f32x2*)(L + L_FIN + lane * 8) = f32x2{-mean * rstd, rstd};
        TS7(4);
        BAR();
        asm volatile("s_barrier" ::: "memory");
        signal(cnt, m * 64 + 32 + p, 512, 32, it + 1, lane);
        pl = wait_count(cnt, (p * 8 + m) * 64 + 32, 32, it + 1, lane);
        TS7(5);
      } else {
        UNROLL for (int i = 25; i < 32; ++i) WI[i] = LDW(wi0, i, 0);
        const v4u ci0u = __builtin_amdgcn_raw_buffer_load_b128(rs, q * 16, OFF_CI + (m * 16 + 2 * w) * 64, 0);
        const v4u ci1u = __builtin_amdgcn_raw_buffer_load_b128(rs, q * 16 + 64, OFF_CI + (m * 16 + 2 * w) * 64, 0);
        BASES(L_XT);
        SCHED();
        asm volatile("s_barrier" ::: "memory");
        TS(10);
        f32x4 acc[4];
        X1DMA(0); X1DMA(1); X1DMA(2); X1DMA(3); X1DMA(4);
        BARV(3);
        TS(11);
        {
          v4u x[80];
          UNROLL for (int i = 0; i < 4; ++i) x[i] = X1F(i);
          SFOR(i, 0, 80, {
            constexpr int ks = i >> 2; constexpr int rt = i & 3;
            if constexpr (i == 12) BARV(3);
            if constexpr (i == 36) BARV(3);
            acc[rt] = mfma(WI[ks], x[i], ks ? acc[rt] : ZERO4);
            if constexpr (i + 4 < 80) x[i + 4] = X1F(i + 4);
            if constexpr (i == 0) X1DMA(5);
            if constexpr (i == 4) X1DMA(6);
            if constexpr (i == 8) X1DMA(7);
            if constexpr (i == 13) X1DMA(8);
            if constexpr (i == 17) X1DMA(9);
            if constexpr (ks >= 6 && ks < 14 && rt == 1) WI[26 + ks] = LDW(wi0, 26 + ks, 0);
            if constexpr (ks >= 14 && rt == 1) WS[ks - 14] = LDW(wsp0, ks - 14, 0);
            SCHED();
          });
        }
        BAR();
        TS(12);
        REFRESH();
        const f32x4 cs = __builtin_bit_cast(f32x4, ci0u);
        float z[4][4], ps[4], pq[4];
        f32x2 st[4];
        UNROLL for (int rt = 0; rt < 4; ++rt) st[rt] = *(LAS f32x2*)(L + L_STAT + (rt * 16 + t) * 8);
        UNROLL for (int rt = 0; rt < 4; ++rt) {
          UNROLL for (int i = 0; i < 4; ++i) z[rt][i] = gelu(__builtin_fmaf(acc[rt][i], st[rt].y, st[rt].x * cs[i]));
          ps[rt] = (z[rt][0] + z[rt][1]) + (z[rt][2] + z[rt][3]);
          pq[rt] = __builtin_fmaf(z[rt][0], z[rt][0], z[rt][1] * z[rt][1]) + __builtin_fmaf(z[rt][2], z[rt][2], z[rt][3] * z[rt][3]);
          WS[6 + 2 * rt] = LDW(wsp0, 6 + 2 * rt, 0);
          WS[7 + 2 * rt] = LDW(wsp0, 7 + 2 * rt, 0);
          SCHED();
        }
        {
          float s2[2], q2[2];
          UNROLL for (int i = 0; i < 2; ++i) {
            auto r0 = __builtin_amdgcn_permlane32_swap(ubits(ps[i]), ubits(ps[2 + i]), false, false);
            s2[i] = fbits(r0[0]) + fbits(r0[1]);
            auto r1 = __builtin_amdgcn_permlane32_swap(ubits(pq[i]), ubits(pq[2 + i]), false, false);
            q2[i] = fbits(r1[0]) + fbits(r1[1]);
          }
          auto r0 = __builtin_amdgcn_permlane16_swap(ubits(s2[0]), ubits(s2[1]), false, false);
          auto r1 = __builtin_amdgcn_permlane16_swap(ubits(q2[0]), ubits(q2[1]), false, false);
          *(LAS f32x2*)(L + L_PART + w * 512 + lane * 8) = f32x2{fbits(r0[0]) + fbits(r0[1]), fbits(r1[0]) + fbits(r1[1])};
        }
        BAR();
        TS(13);
        REFRESH();
        BASES(L_XT);
        f32x4 acc1[4];
#define P3_Z1(k0, k1, w0) do { \
          v4u x[80]; \
          UNROLL for (int i = 4 * (k0); i < 4 * (k0) + 4; ++i) x[i] = X1F(i); \
          SFOR(i, 4 * (k0), 4 * (k1), { \
            acc1[i & 3] = mfma(WI[20 + (i >> 2)], x[i], i >= 4 ? acc1[i & 3] : ZERO4); \
            if constexpr (i + 4 < 4 * (k1)) x[i + 4] = X1F(i + 4); \
            if constexpr ((i & 3) == 1 && (w0) + (i >> 2) - (k0) < 32) WS[(w0) + (i >> 2) - (k0)] = LDW(wsp0, (w0) + (i >> 2) - (k0), 0); \
            SCHED(); \
          }); } while (0)
        P3_Z1(0, 10, 14);
        BAR();
        TS(14);
        REFRESH();
        LAS char* tr = L + L_TR + w * 2048;
        UNROLL for (int rt = 0; rt < 4; ++rt) {
          const f32x2 fs = *(LAS f32x2*)(L + L_FIN + (rt * 16 + t) * 8);
          *(LAS v2u*)(tr + (rt * 16 + t) * 32 + q * 8) =
              v2u{pack2(__builtin_fmaf(z[rt][0], fs.y, fs.x), __builtin_fmaf(z[rt][1], fs.y, fs.x)),
                  pack2(__builtin_fmaf(z[rt][2], fs.y, fs.x), __builtin_fmaf(z[rt][3], fs.y, fs.x))};
        }
        asm volatile("s_waitcnt lgkmcnt(0)" ::: "memory");
        const u32 zo = (w < 6 || m < 6) ? OFF_Z2 + it * ZSZ + ((m * 7 + w) * 64 + 2 * p) * 1024 : OOB;
        UNROLL for (int k2 = 0; k2 < 2; ++k2) {
          LAS char* src = tr + (32 * k2 + 8 * q + (t >> 2)) * 32 + (t & 3) * 8;
          const v2u lo = __builtin_bit_cast(v2u, __builtin_amdgcn_ds_read_tr16_b64_v4i16((LAS s16x4*)src));
          const v2u hi = __builtin_bit_cast(v2u, __builtin_amdgcn_ds_read_tr16_b64_v4i16((LAS s16x4*)(src + 128)));
          st128(v4u{lo.x, lo.y, hi.x, hi.y}, rs, lane16 + k2 * 1024, zo);
        }
        SCHED();
        BASES(L_XT);
        P3_Z1(10, 16, 24);
        BARV(6);
        TS(15);
        REFRESH();
        BASES(L_XT);
        P3_Z1(16, 20, 30);
        const f32x4 cs1 = __builtin_bit_cast(f32x4, ci1u);
        const u32 wg = OFF_WOUT + (m * 135 + 79 + 8 * w) * 1024, wl = lds0 + L_XT + (79 + 8 * w) * 1024;
        SFOR(rt, 0, 4, {
          if (rt > 0 || w > 0) dma16<false, (rt & 1) * 2048>(srd, lane16, wg + (rt >> 1) * 4096, wl + (rt >> 1) * 4096);
          dma16<false, (rt & 1) * 2048 + 1024>(srd, lane16, wg + (rt >> 1) * 4096, wl + (rt >> 1) * 4096);
          float g[4];
          UNROLL for (int i = 0; i < 4; ++i) g[i] = gelu(__builtin_fmaf(acc1[rt][i], st[rt].y, st[rt].x * cs1[i]));
          *(LAS v2u*)(L + L_Z1 + (w * 4 + rt) * 512 + lane * 8) = v2u{pack2(g[0], g[1]), pack2(g[2], g[3])};
          SCHED();
        });
      }
    }
    REFRESH();
    {
      u32 zi = OFF_Z2 + it * ZSZ + (m * 7 * 64 + 8 * w) * 1024;
      u32 lane16z = m < 6 ? lane16 : OOB;
      asm volatile("" : "+s"(zi));
      asm volatile("" : "+v"(lane16z));
      SCHED();
      BAR();
      TS(16);
      v4u Z[56];
#define ZLD(f) Z[f] = LDV((f) >= 48 ? lane16z : lane16, zi + ((f) >> 3) * 65536, (f) & 7, SC1)
      UNROLL for (int f = 0; f < RING; ++f) ZLD(f);
      SCHED();
      f32x4 acc[2][4];
      f32x2 part[8];
      u32 z1;
      LAS char* redw = L + w * 4096 + lane16;
      LAS char* redr = L + r * 1024 + lane16;
      LAS char* z1r = L + L_Z1 + r * 512 + lane * 8;
      const float bias = *(LAS float*)(L + L_BIAS + (16 * r + t) * 4);
      const u32 uo = OFF_U + it * AT27 + (4 * p + r) * 27 * 1024;
      SFOR(f, 0, 56, {
        constexpr int c = f >> 3; constexpr int ks = f & 7;
        UNROLL for (int rt = 0; rt < 4; ++rt) acc[c & 1][rt] = mfma(Z[f], WS[ks * 4 + rt], ks ? acc[c & 1][rt] : ZERO4);
        if constexpr (ks == 0) TSX(c);
        if constexpr (f == 55) TSX(7);
        if constexpr (W7 && f == 1) poll_done(pl);
        if constexpr (f + RING < 56) ZLD(f + RING);
        if constexpr (f + RING >= 56 && f + RING < 61) Q0[f + RING - 56] = LDW(wq0, f + RING - 56, 0);
        if constexpr (c > 0) {
          if constexpr (ks < 4) *(LAS f32x4*)(redw + ((c - 1) & 1) * 32768 + ks * 1024) = acc[(c - 1) & 1][ks];
          if constexpr (ks == 5) {
            SCHED(); BAR();
            UNROLL for (int i = 0; i < 8; ++i) part[i] = *(LAS f32x2*)(redr + kh * 8 + ((c - 1) & 1) * 32768 + i * 4096);
            z1 = *(LAS u32*)(z1r + kh * 4 + (c - 1) * 2048);
            if constexpr (c == 5) {
              const v2u lo = *(LAS v2u*)(z1r + kh * 4096), hi = *(LAS v2u*)(z1r + kh * 4096 + 2048);
              st128(v4u{lo.x, lo.y, hi.x, hi.y}, rs, lane16, uo + (f_pair(m, kh) >> 5) * 1024);
            }
          }
          if constexpr (ks == 7) {
            const float s0 = add1(add1(add1(add1(part[0].x, part[1].x), add1(part[2].x, part[3].x)), add1(add1(part[4].x, part[5].x), add1(part[6].x, part[7].x))), bias);
            const float s1 = add1(add1(add1(add1(part[0].y, part[1].y), add1(part[2].y, part[3].y)), add1(add1(part[4].y, part[5].y), add1(part[6].y, part[7].y))), bias);
            *(LAS u32*)(z1r + kh * 4 + (c - 1) * 2048) = pack2(mul1(bf2f(z1), s0), mul1(bfhi(z1), s1));
          }
        }
        SCHED();
      });
      UNROLL for (int rt = 0; rt < 4; ++rt) *(LAS f32x4*)(redw + rt * 1024) = acc[0][rt];
      BAR();
      TSX(8);
      if (kh == 1) {
        f32x4 pt[8];
        UNROLL for (int i = 0; i < 8; ++i) pt[i] = *(LAS f32x4*)(redr + i * 4096);
        const v2u zz = *(LAS v2u*)(z1r + 6 * 2048);
        const f32x4 sum = ((pt[0] + pt[1]) + (pt[2] + pt[3])) + ((pt[4] + pt[5]) + (pt[6] + pt[7]));
        const v2u u_ = {pack2(bf2f(zz.x) * (sum[0] + bias), bfhi(zz.x) * (sum[1] + bias)), pack2(bf2f(zz.y) * (sum[2] + bias), bfhi(zz.y) * (sum[3] + bias))};
        __builtin_amdgcn_raw_buffer_store_b64(u_, rs, m < 6 ? vS + uo + (f_single(m) >> 5) * 1024 + ((f_single(m) & 31) >> 3) * 256 : OOB, 0, SC1);
        WAITV(0);
      } else {
        const v2u lo = *(LAS v2u*)(z1r + 4 * 2048), hi = *(LAS v2u*)(z1r + 5 * 2048);
        st128(v4u{lo.x, lo.y, hi.x, hi.y}, rs, lane16, uo + (f_pair(m, 2) >> 5) * 1024);
        SCHED();
        const u32 g = OFF_WOUT + (m * 135 + 32 + 12 * w) * 1024, l = lds0 + L_XT + (32 + 12 * w) * 1024;
        SFOR(i, 0, 12, { dma16<false, (i & 3) * 1024>(srd, lane16, g + (i >> 2) * 4096, l + (i >> 2) * 4096); });
        WAITV(12);
      }
      TS(17);
      BAR();
    }
    TS(18);
    REFRESH();
    {
      if constexpr (W7) {
        signal(cnt, p * 512 + m, 64, 8, 4 * it + 3, lane);
        pl = wait_count(cnt, (p * 8 + m) * 64, 8, 4 * it + 3, lane);
        TS7(6);
      } else {
        const u32 g = OFF_WOUT + (m * 135 + 5 * w) * 1024, l = lds0 + L_XT + 5 * w * 1024;
        dma16<false, 0>(srd, lane16, g, l); dma16<false, 1024>(srd, lane16, g, l);
        if (w < 6) { dma16<false, 2048>(srd, lane16, g, l); dma16<false, 3072>(srd, lane16, g, l); dma16<false, 0>(srd, lane16, g + 4096, l + 4096); }
        SCHED();
        UNROLL for (int i = 0; i < 2; ++i) Q1[i] = LDW(wq1, i, 0);
        SCHED();
        WAITV(2);
      }
      u32 ao = OFF_U + it * AT27 + ((4 * p + r) * 27 + 14 * kh) * 1024;
      u32 lane16k = kh ? OOB : lane16;
      asm volatile("" : "+s"(ao));
      asm volatile("" : "+v"(lane16k));
      BASES(L_XT + kh * 14 * 1024);
      SCHED();
      asm volatile("s_barrier" ::: "memory");
      TS(19);
      v4u A[14], Bf[70];
      UNROLL for (int i = 0; i < 5; ++i) A[i] = LDW(ao, i, SC1);
#define WTF(i) FRAG(((i) % 5) * 27 + (i) / 5)
      UNROLL for (int i = 0; i < 8; ++i) Bf[i] = WTF(i);
      SCHED();
      f32x4 acc[5];
      SFOR(i, 0, 70, {
        acc[i % 5] = mfma(Bf[i], A[i / 5], i < 5 ? ZERO4 : acc[i % 5]);
        if constexpr (W7 && i == 1) poll_done(pl);
        if constexpr (i + 8 < 69) Bf[i + 8] = WTF(i + 8);
        if constexpr (i + 8 == 69) Bf[69] = *(LAS v4u*)(B1 + (4 * 27 + 13 - 64) * 1024 - kh * 1024);
        if constexpr (i % 5 == 0 && i < 40) A[i / 5 + 5] = LDW(ao, i / 5 + 5, SC1);
        if constexpr (i == 40) A[13] = LDV(lane16k, ao, 13, SC1);
        if constexpr (i % 5 == 0 && i >= 45 && i < 60) Q0[5 + (i - 45) / 5] = LDW(wq0, 5 + (i - 45) / 5, 0);
        SCHED();
      });
      LAS char* ex = L + L_EXCH + r * 5 * 1024 + lane16;
      if (kh) {
        *(LAS f32x4*)(ex) = acc[0]; *(LAS f32x4*)(ex + 1024) = acc[1]; *(LAS f32x4*)(ex + 4096) = acc[4];
      } else {
        *(LAS f32x4*)(ex + 2048) = acc[2]; *(LAS f32x4*)(ex + 3072) = acc[3];
      }
      BAR();
      TS(20);
      REFRESH();
      ex = L + L_EXCH + r * 5 * 1024 + lane16;
      const f32x4 a0 = (kh ? acc[2] : acc[0]) + *(LAS f32x4*)(ex + kh * 2048);
      const f32x4 a1 = (kh ? acc[3] : acc[1]) + *(LAS f32x4*)(ex + kh * 2048 + 1024);
      const f32x4 a4 = acc[4] + *(LAS f32x4*)(ex + 4096);
      resP = v4u{pack2(bf2f(resP.x) + a0[0], bfhi(resP.x) + a0[1]), pack2(bf2f(resP.y) + a0[2], bfhi(resP.y) + a0[3]),
                 pack2(bf2f(resP.z) + a1[0], bfhi(resP.z) + a1[1]), pack2(bf2f(resP.w) + a1[2], bfhi(resP.w) + a1[3])};
      resS = v2u{pack2(bf2f(resS.x) + a4[0], bfhi(resS.x) + a4[1]), pack2(bf2f(resS.y) + a4[2], bfhi(resS.y) + a4[3])};
      if (it == 1) {
        const u32 row = (64 * p + 16 * r + t) * (D * 2);
        __builtin_amdgcn_raw_buffer_store_b128(resP, rs_out, row + (d_pair(m, kh) + 8 * q) * 2, 0, 0);
        __builtin_amdgcn_raw_buffer_store_b64(resS, rs_out, kh ? OOB : row + (d_single(m) + 4 * q) * 2, 0, 0);
        TS(21);
        asm volatile("s_endpgm" ::: "memory");
      }
      st128(resP, rs, lane16, OFF_X + XSZ + xoffP);
      __builtin_amdgcn_raw_buffer_store_b64(resS, rs, kh ? OOB : vS + OFF_X + XSZ + xoffS, 0, SC1);
      SX_PUBLISH(2);
      SCHED();
      UNROLL for (int i = 8; i < 10; ++i) Q0[i] = LDW(wq0, i, 0);
      if (!W7) { UNROLL for (int i = 2; i < 4; ++i) Q1[i] = LDW(wq1, i, 0); }
      SCHED();
      BARV(W7 ? 2 : 4);
      TS(21);
      if constexpr (W7) {
        signal(cnt, p * 512 + m + (lane & 8), 64, p < 31 ? 16 : 8, 4, lane);
        pl = wait_count(cnt, (p * 8 + m) * 64, p > 0 ? 16 : 8, 4, lane);
        TS7(7);
      } else {
        UNROLL for (int i = 4; i < 10; ++i) Q1[i] = LDW(wq1, i, 0);
      }
      SCHED();
      asm volatile("s_barrier" ::: "memory");
    }
  }
}

__global__ void __launch_bounds__(512, 2) mega_kernel(MegaArgs a) {
  extern __shared__ __attribute__((aligned(16))) char smem[];
  const int w = __builtin_amdgcn_readfirstlane(threadIdx.x >> 6);
  if (w == 7) __builtin_amdgcn_s_setprio(3);
  if (w == 7) mega_body<true>(a, (LAS char*)smem, w);
  else mega_body<false>(a, (LAS char*)smem, w);
}

}

extern "C" void kernel_launch(void* const* d_in, const int* in_sizes, int n_in, void* d_out, int out_size, void* d_ws,
                              size_t ws_size, hipStream_t stream) {
  PrepArgs pa;
  pa.x = (const u16*)d_in[0];
  pa.wqkv = (const u16*)d_in[1];
  pa.wo = (const u16*)d_in[2];
  pa.win = (const u16*)d_in[4];
  pa.wsp = (const u16*)d_in[5];
  pa.wout = (const u16*)d_in[7];
  pa.ws = (char*)d_ws;
  prep_kernel<<<PB_END, 256, 0, stream>>>(pa);
  MegaArgs ma;
  ma.ws = (char*)d_ws;
  ma.bsp = (const u16*)d_in[6];
  ma.out = (u16*)d_out;
  (void)hipFuncSetAttribute((const void*)mega_kernel, hipFuncAttributeMaxDynamicSharedMemorySize, LDS_BYTES);
  mega_kernel<<<256, 512, LDS_BYTES, stream>>>(ma);
}
```

```cpp
#include <hip/hip_runtime.h>
#include <stdint.h>
#include <utility>

typedef unsigned short u16;
typedef unsigned int u32;
typedef unsigned long long u64;
typedef u32 v4u __attribute__((ext_vector_type(4)));
typedef u32 v2u __attribute__((ext_vector_type(2)));
typedef float f32x4 __attribute__((ext_vector_type(4)));
typedef float f32x2 __attribute__((ext_vector_type(2)));
typedef __bf16 bf16x8 __attribute__((ext_vector_type(8)));
typedef __bf16 bf16x2 __attribute__((ext_vector_type(2)));
typedef short s16x4 __attribute__((ext_vector_type(4)));
#define GAS __attribute__((address_space(1)))
#define LAS __attribute__((address_space(3)))

namespace {

constexpr int S = 2048, D = 640, FH = 864;
constexpr u32 OFF_CNT = 0;
constexpr u32 OFF_G = 131072;
constexpr u32 OFF_ST = OFF_G + 4096;
constexpr u32 OFF_WQ = OFF_ST + 2 * 32 * 8 * 64 * 16;
constexpr u32 OFF_WO = OFF_WQ + 8 * 16 * 20 * 1024;
constexpr u32 OFF_WI = OFF_WO + 8 * 5 * 20 * 1024;
constexpr u32 OFF_WOUT = OFF_WI + 8 * 16 * 20 * 1024;
constexpr u32 OFF_WSP = OFF_WOUT + 8 * 5 * 27 * 1024;
constexpr u32 AT20 = 128 * 20 * 1024, AT27 = 128 * 27 * 1024, ZSZ = 8 * 7 * 64 * 1024;
constexpr u32 OFF_X2 = OFF_WSP + 32 * 8 * 32 * 1024;
constexpr u32 OFF_O = OFF_X2 + AT20;
constexpr u32 OFF_X1 = OFF_O + 2 * AT20;
constexpr u32 OFF_Z2 = OFF_X1 + 2 * AT20;
constexpr u32 OFF_U = OFF_Z2 + 2 * ZSZ;
constexpr u32 SXSZ = 128 * 2048;
constexpr u32 OFF_SX = OFF_U + 2 * AT27;
constexpr u32 WS_BYTES = OFF_SX + 3 * SXSZ;

constexpr u32 L_XT = 0;
constexpr u32 L_STG = 102400;
constexpr u32 L_STQ = L_STG + 24576, L_STU = L_STQ + 10240;
constexpr u32 L_QKV = 102400;
constexpr u32 QKV_STRIDE = 488;
constexpr u32 L_EXCH = 138240;
constexpr u32 L_PART = 138240;
constexpr u32 L_FIN = 160768;
constexpr u32 L_TR = 143360;
constexpr u32 L_Z1 = 143360;
constexpr u32 L_SX = 143360;
constexpr u32 L_STAT = 158720;
constexpr u32 L_BIAS = 159360;
constexpr u32 L_CQ = 159744;
constexpr u32 LDS_BYTES = 160 * 1024;

constexpr int SC1 = 16;
constexpr u32 SPIN_CAP = 1u << 18;

__device__ __forceinline__ float bf2f(u32 lo16) { return __builtin_bit_cast(float, lo16 << 16); }
__device__ __forceinline__ float bfhi(u32 x) { return __builtin_bit_cast(float, x & 0xffff0000u); }
__device__ __forceinline__ u32 pack2(float a, float b) {
  f32x2 v = {a, b};
  return __builtin_bit_cast(u32, __builtin_convertvector(v, bf16x2));
}
__device__ __forceinline__ f32x4 mfma(v4u a, v4u b, f32x4 c) {
  f32x4 d = __builtin_amdgcn_mfma_f32_16x16x32_bf16(__builtin_bit_cast(bf16x8, a), __builtin_bit_cast(bf16x8, b), c, 0, 0, 0);
  asm volatile("" : "+v"(d));
  return d;
}
template <bool INIT>
__device__ __forceinline__ void mma(f32x4& acc, v4u a, v4u b) {
  if (INIT) asm volatile("v_mfma_f32_16x16x32_bf16 %0, %1, %2, 0" : "=&v"(acc) : "v"(a), "v"(b));
  else asm volatile("v_mfma_f32_16x16x32_bf16 %0, %1, %2, %0" : "+v"(acc) : "v"(a), "v"(b));
}
__device__ __forceinline__ float dot2(u32 a, u32 b, float c) {
  return __builtin_amdgcn_fdot2_f32_bf16(__builtin_bit_cast(bf16x2, a), __builtin_bit_cast(bf16x2, b), c, false);
}
__device__ __forceinline__ float fbits(u32 x) { return __builtin_bit_cast(float, x); }
__device__ __forceinline__ u32 ubits(float x) { return __builtin_bit_cast(u32, x); }
__device__ __forceinline__ float xsum32(float x) {
  auto r = __builtin_amdgcn_permlane32_swap(ubits(x), ubits(x), false, false);
  return fbits(r[0]) + fbits(r[1]);
}
__device__ __forceinline__ float xsum16(float x) {
  auto r = __builtin_amdgcn_permlane16_swap(ubits(x), ubits(x), false, false);
  return fbits(r[0]) + fbits(r[1]);
}
__device__ __forceinline__ void st128(v4u v, __amdgpu_buffer_rsrc_t rs, u32 voff, u32 soff) {
  __builtin_amdgcn_raw_buffer_store_b128(v, rs, voff + soff, 0, SC1);
}
__device__ __forceinline__ float add1(float x, float y) { float z; asm("v_add_f32 %0, %1, %2" : "=v"(z) : "v"(x), "v"(y)); return z; }
__device__ __forceinline__ float mul1(float x, float y) { float z; asm("v_mul_f32 %0, %1, %2" : "=v"(z) : "v"(x), "v"(y)); return z; }
__device__ __forceinline__ void mfma_settle() { asm volatile("s_nop 7\n\ts_nop 1" ::: "memory"); }
__device__ __forceinline__ v2u fold4(f32x4 acc, f32x4 cs, f32x2 st) {
  float t0, t1, t2, t3;
  u32 lo, hi;
  asm volatile(
      "v_mul_f32 %2, %14, %10\n\tv_mul_f32 %3, %14, %11\n\tv_mul_f32 %4, %14, %12\n\tv_mul_f32 %5, %14, %13\n\t"
      "v_fmac_f32 %2, %6, %15\n\tv_fmac_f32 %3, %7, %15\n\tv_fmac_f32 %4, %8, %15\n\tv_fmac_f32 %5, %9, %15\n\t"
      "v_cvt_pk_bf16_f32 %0, %2, %3\n\tv_cvt_pk_bf16_f32 %1, %4, %5"
      : "=&v"(lo), "=&v"(hi), "=&v"(t0), "=&v"(t1), "=&v"(t2), "=&v"(t3)
      : "v"(acc[0]), "v"(acc[1]), "v"(acc[2]), "v"(acc[3]), "v"(cs[0]), "v"(cs[1]), "v"(cs[2]), "v"(cs[3]), "v"(st.x), "v"(st.y));
  return v2u{lo, hi};
}
__device__ __forceinline__ void gelu4(f32x4 acc, f32x4 cs, f32x2 st, float c1, float (&z)[4]) {
  float e0, e1, e2, e3;
  asm volatile(
      "v_mul_f32 %0, %16, %12\n\tv_mul_f32 %1, %16, %13\n\tv_mul_f32 %2, %16, %14\n\tv_mul_f32 %3, %16, %15\n\t"
      "v_fmac_f32 %0, %8, %17\n\tv_fmac_f32 %1, %9, %17\n\tv_fmac_f32 %2, %10, %17\n\tv_fmac_f32 %3, %11, %17\n\t"
      "v_mul_f32 %4, %0, %0\n\tv_mul_f32 %5, %1, %1\n\tv_mul_f32 %6, %2, %2\n\tv_mul_f32 %7, %3, %3\n\t"
      "v_fmamk_f32 %4, %4, 0xbdd2d3e8, %18\n\tv_fmamk_f32 %5, %5, 0xbdd2d3e8, %18\n\tv_fmamk_f32 %6, %6, 0xbdd2d3e8, %18\n\tv_fmamk_f32 %7, %7, 0xbdd2d3e8, %18\n\t"
      "v_mul_f32 %4, %0, %4\n\tv_mul_f32 %5, %1, %5\n\tv_mul_f32 %6, %2, %6\n\tv_mul_f32 %7, %3, %7\n\t"
      "v_exp_f32 %4, %4\n\tv_exp_f32 %5, %5\n\tv_exp_f32 %6, %6\n\tv_exp_f32 %7, %7\n\t"
      "v_add_f32 %4, 1.0, %4\n\tv_add_f32 %5, 1.0, %5\n\tv_add_f32 %6, 1.0, %6\n\tv_add_f32 %7, 1.0, %7\n\t"
      "v_rcp_f32 %4, %4\n\tv_rcp_f32 %5, %5\n\tv_rcp_f32 %6, %6\n\tv_rcp_f32 %7, %7\n\t"
      "v_mul_f32 %0, %0, %4\n\tv_mul_f32 %1, %1, %5\n\tv_mul_f32 %2, %2, %6\n\tv_mul_f32 %3, %3, %7"
      : "=&v"(z[0]), "=&v"(z[1]), "=&v"(z[2]), "=&v"(z[3]), "=&v"(e0), "=&v"(e1), "=&v"(e2), "=&v"(e3)
      : "v"(acc[0]), "v"(acc[1]), "v"(acc[2]), "v"(acc[3]), "v"(cs[0]), "v"(cs[1]), "v"(cs[2]), "v"(cs[3]), "v"(st.x), "v"(st.y), "v"(c1));
}
template <int K>
__device__ __forceinline__ void gelu_row(const f32x4& acc, const f32x4& cs, f32x2 st, float c1, float (&z)[4], float (&e)[4], float& s, float& sq) {
  if constexpr (K == 0) asm volatile("v_mul_f32 %0, %8, %4\n\tv_mul_f32 %1, %8, %5\n\tv_mul_f32 %2, %8, %6\n\tv_mul_f32 %3, %8, %7"
      : "=&v"(z[0]), "=&v"(z[1]), "=&v"(z[2]), "=&v"(z[3]) : "v"(cs[0]), "v"(cs[1]), "v"(cs[2]), "v"(cs[3]), "v"(st.x));
  if constexpr (K == 1) asm volatile("v_fmac_f32 %0, %4, %8\n\tv_fmac_f32 %1, %5, %8\n\tv_fmac_f32 %2, %6, %8\n\tv_fmac_f32 %3, %7, %8"
      : "+v"(z[0]), "+v"(z[1]), "+v"(z[2]), "+v"(z[3]) : "v"(acc[0]), "v"(acc[1]), "v"(acc[2]), "v"(acc[3]), "v"(st.y));
  if constexpr (K == 2) asm volatile("v_mul_f32 %0, %4, %4\n\tv_mul_f32 %1, %5, %5\n\tv_mul_f32 %2, %6, %6\n\tv_mul_f32 %3, %7, %7"
      : "=&v"(e[0]), "=&v"(e[1]), "=&v"(e[2]), "=&v"(e[3]) : "v"(z[0]), "v"(z[1]), "v"(z[2]), "v"(z[3]));
  if constexpr (K == 3) asm volatile("v_fmamk_f32 %0, %0, 0xbdd2d3e8, %4\n\tv_fmamk_f32 %1, %1, 0xbdd2d3e8, %4\n\tv_fmamk_f32 %2, %2, 0xbdd2d3e8, %4\n\tv_fmamk_f32 %3, %3, 0xbdd2d3e8, %4"
      : "+v"(e[0]), "+v"(e[1]), "+v"(e[2]), "+v"(e[3]) : "v"(c1));
  if constexpr (K == 4) asm volatile("v_mul_f32 %0, %4, %0\n\tv_mul_f32 %1, %5, %1\n\tv_mul_f32 %2, %6, %2\n\tv_mul_f32 %3, %7, %3"
      : "+v"(e[0]), "+v"(e[1]), "+v"(e[2]), "+v"(e[3]) : "v"(z[0]), "v"(z[1]), "v"(z[2]), "v"(z[3]));
  if constexpr (K == 5) asm volatile("v_exp_f32 %0, %0\n\tv_exp_f32 %1, %1\n\tv_exp_f32 %2, %2\n\tv_exp_f32 %3, %3" : "+v"(e[0]), "+v"(e[1]), "+v"(e[2]), "+v"(e[3]));
  if constexpr (K == 6) asm volatile("v_add_f32 %0, 1.0, %0\n\tv_add_f32 %1, 1.0, %1\n\tv_add_f32 %2, 1.0, %2\n\tv_add_f32 %3, 1.0, %3" : "+v"(e[0]), "+v"(e[1]), "+v"(e[2]), "+v"(e[3]));
  if constexpr (K == 7) asm volatile("v_rcp_f32 %0, %0\n\tv_rcp_f32 %1, %1\n\tv_rcp_f32 %2, %2\n\tv_rcp_f32 %3, %3" : "+v"(e[0]), "+v"(e[1]), "+v"(e[2]), "+v"(e[3]));
  if constexpr (K == 8) asm volatile("v_mul_f32 %0, %0, %4\n\tv_mul_f32 %1, %1, %5\n\tv_mul_f32 %2, %2, %6\n\tv_mul_f32 %3, %3, %7"
      : "+v"(z[0]), "+v"(z[1]), "+v"(z[2]), "+v"(z[3]) : "v"(e[0]), "v"(e[1]), "v"(e[2]), "v"(e[3]));
  if constexpr (K == 9) asm volatile("v_add_f32 %0, %3, %4\n\tv_mul_f32 %1, %3, %3\n\tv_add_f32 %2, %5, %6\n\tv_fmac_f32 %1, %4, %4"
      : "=&v"(s), "=&v"(sq), "=&v"(e[0]) : "v"(z[0]), "v"(z[1]), "v"(z[2]), "v"(z[3]));
  if constexpr (K == 10) asm volatile("v_add_f32 %0, %0, %2\n\tv_fmac_f32 %1, %3, %3\n\tv_fmac_f32 %1, %4, %4" : "+v"(s), "+v"(sq) : "v"(e[0]), "v"(z[2]), "v"(z[3]));
}
__device__ __forceinline__ v2u fold4b(f32x4 acc, f32x4 t, float rstd) {
  u32 lo, hi;
  asm volatile("v_fmac_f32 %2, %6, %10\n\tv_fmac_f32 %3, %7, %10\n\tv_fmac_f32 %4, %8, %10\n\tv_fmac_f32 %5, %9, %10\n\t"
               "v_cvt_pk_bf16_f32 %0, %2, %3\n\tv_cvt_pk_bf16_f32 %1, %4, %5"
               : "=&v"(lo), "=&v"(hi), "+v"(t[0]), "+v"(t[1]), "+v"(t[2]), "+v"(t[3])
               : "v"(acc[0]), "v"(acc[1]), "v"(acc[2]), "v"(acc[3]), "v"(rstd));
  return v2u{lo, hi};
}
__device__ __forceinline__ float max1(float x, float y) { float z; asm("v_max_f32 %0, %1, %2\n\ts_nop 1" : "=v"(z) : "v"(x), "v"(y)); return z; }
__device__ __forceinline__ u32 bseli(u32 mask, u32 x, u32 y) { return (x & mask) | (y & ~mask); }
__device__ __forceinline__ float bsel(u32 mask, float x, float y) { return fbits(bseli(mask, ubits(x), ubits(y))); }
__device__ __forceinline__ f32x2 ln_pair(float s, float sq, float rn) {
  float mean = s * rn;
  asm volatile("" : "+v"(mean));
  const float rstd = __builtin_amdgcn_rsqf(__builtin_fmaxf(__builtin_fmaf(sq, rn, -mean * mean), 0.f) + 1e-5f);
  return f32x2{-mean * rstd, rstd};
}
__device__ __forceinline__ v2u addpack(f32x4 a, f32x4 b) { return v2u{pack2(add1(a[0], b[0]), add1(a[1], b[1])), pack2(add1(a[2], b[2]), add1(a[3], b[3]))}; }
__device__ __forceinline__ v2u norm4(const float (&z)[4], f32x2 f) {
  float t0, t1, t2, t3;
  u32 lo, hi;
  asm volatile("v_fma_f32 %2, %6, %11, %10\n\tv_fma_f32 %3, %7, %11, %10\n\tv_fma_f32 %4, %8, %11, %10\n\tv_fma_f32 %5, %9, %11, %10\n\t"
               "v_cvt_pk_bf16_f32 %0, %2, %3\n\tv_cvt_pk_bf16_f32 %1, %4, %5"
               : "=&v"(lo), "=&v"(hi), "=&v"(t0), "=&v"(t1), "=&v"(t2), "=&v"(t3)
               : "v"(z[0]), "v"(z[1]), "v"(z[2]), "v"(z[3]), "v"(f.x), "v"(f.y));
  return v2u{lo, hi};
}
__device__ __forceinline__ void sum4x4(f32x4 (&p)[4], float (&s)[4]) {
  asm volatile("v_add_f32 %0, %8, %12\n\tv_add_f32 %1, %9, %13\n\tv_add_f32 %2, %10, %14\n\tv_add_f32 %3, %11, %15\n\t"
               "v_add_f32 %4, %4, %16\n\tv_add_f32 %5, %5, %17\n\tv_add_f32 %6, %6, %18\n\tv_add_f32 %7, %7, %19\n\t"
               "v_add_f32 %0, %0, %4\n\tv_add_f32 %2, %2, %6\n\tv_add_f32 %1, %1, %5\n\tv_add_f32 %3, %3, %7\n\ts_nop 1"
               : "=&v"(s[0]), "=&v"(s[1]), "=&v"(s[2]), "=&v"(s[3]), "+v"(p[2][0]), "+v"(p[2][1]), "+v"(p[2][2]), "+v"(p[2][3])
               : "v"(p[0][0]), "v"(p[0][1]), "v"(p[0][2]), "v"(p[0][3]), "v"(p[1][0]), "v"(p[1][1]), "v"(p[1][2]), "v"(p[1][3]),
                 "v"(p[3][0]), "v"(p[3][1]), "v"(p[3][2]), "v"(p[3][3]));
}
__device__ __forceinline__ void fence() { asm volatile("" ::: "memory"); }
__device__ __forceinline__ float addv(float x, float y) { float z; asm volatile("v_add_f32 %0, %1, %2" : "=v"(z) : "v"(x), "v"(y)); return z; }
__device__ __forceinline__ float mulv(float x, float y) { float z; asm volatile("v_mul_f32 %0, %1, %2" : "=v"(z) : "v"(x), "v"(y)); return z; }
__device__ __forceinline__ void pin(v4u& v) { asm volatile("" : "+v"(v)); }
__device__ __forceinline__ void keepalive(f32x4 v) { asm volatile("" :: "v"(v)); }
#define WAITV(n) asm volatile("s_waitcnt vmcnt(%0)" ::"n"(n) : "memory")
#define BAR() asm volatile("s_waitcnt lgkmcnt(0)\n\ts_barrier" ::: "memory")
#define BARV(n) asm volatile("s_waitcnt vmcnt(%0)\n\ts_barrier" ::"n"(n) : "memory")
#define BARVL(n) asm volatile("s_waitcnt vmcnt(%0) lgkmcnt(0)\n\ts_barrier" ::"n"(n) : "memory")
#define PAUSE(n) do { if constexpr ((n) > 0) asm volatile("s_sleep %0" :: "n"(n) : "memory"); } while (0)
#define SCHED() __builtin_amdgcn_sched_barrier(0)

template <int COH, int IMM>
__device__ __forceinline__ void dma16(v4u srd, u32 voff, u32 soff, u32 ldsoff) {
  if (COH == 2)
    asm volatile("s_mov_b32 m0, %0\n\ts_nop 0\n\tbuffer_load_dwordx4 %1, %2, %3 offen offset:%4 nt lds" ::"s"(ldsoff), "v"(voff), "s"(srd), "s"(soff), "n"(IMM) : "memory");
  else if (COH == 1)
    asm volatile("s_mov_b32 m0, %0\n\ts_nop 0\n\tbuffer_load_dwordx4 %1, %2, %3 offen offset:%4 sc1 lds" ::"s"(ldsoff), "v"(voff), "s"(srd), "s"(soff), "n"(IMM) : "memory");
  else
    asm volatile("s_mov_b32 m0, %0\n\ts_nop 0\n\tbuffer_load_dwordx4 %1, %2, %3 offen offset:%4 lds" ::"s"(ldsoff), "v"(voff), "s"(srd), "s"(soff), "n"(IMM) : "memory");
}

__host__ __device__ constexpr int d_pair(int m, int P) { return 80 * m + ((m & 1) ? 16 : 0) + 32 * P; }
__host__ __device__ constexpr int d_single(int m) { return 80 * m + ((m & 1) ? 0 : 64); }
__host__ __device__ constexpr int f_base(int m) { return m < 7 ? 112 * m : 768; }
__host__ __device__ constexpr int f_pair(int m, int P) { return f_base(m) + (((m & 1) && m < 6) ? 16 : 0) + 32 * P; }
__host__ __device__ constexpr int f_single(int m) { return f_base(m) + ((m & 1) ? 0 : 96); }

__device__ __forceinline__ int map_p(int b) { return ((b & 7) >> 1) * 8 + (b >> 5); }
__device__ __forceinline__ int map_m(int b) { return (b & 1) * 4 + ((b >> 3) & 3); }
__device__ __forceinline__ int o_row(int k) {
  const int id = k >> 3, j = k & 7, m = id / 10, c = id % 10;
  if (c < 8) return 80 * m + 20 * (c >> 1) + 8 * (c & 1) + j;
  return 80 * m + 20 * (2 * (c - 8) + (j >> 2)) + 16 + (j & 3);
}

struct MegaArgs {
  char* ws;
  const u16 *x, *wqkv, *wo, *win, *wsp, *bsp, *wout;
  u16* out;
};

__device__ __forceinline__ void signal(v4u srd, u32 voff, u64 val) {
  asm volatile("buffer_store_dwordx2 %0, %1, %2, 0 offen sc1" ::"v"(val), "v"(voff), "s"(srd) : "memory");
}
__device__ __forceinline__ void wait_phase(v4u srd, u32 voff, u64 lo, u64 hi) {
  u64 v, m;
  u32 left = SPIN_CAP;
  asm volatile(
      "1:\n\t"
      "buffer_load_dwordx2 %0, %3, %4, 0 offen sc1\n\t"
      "s_sub_u32 %2, %2, 1\n\t"
      "s_cbranch_scc1 2f\n\t"
      "s_waitcnt vmcnt(0)\n\t"
      "v_cmp_gt_u64 vcc, %5, %0\n\t"
      "v_cmp_lt_u64 %1, %6, %0\n\t"
      "s_or_b64 vcc, vcc, %1\n\t"
      "s_cbranch_scc1 1b\n"
      "2:\n\t"
      "s_waitcnt vmcnt(0)"
      : "=&v"(v), "=&s"(m), "+s"(left)
      : "v"(voff), "s"(srd), "s"(lo), "s"(hi)
      : "vcc", "scc", "memory");
}
__device__ __forceinline__ v4u make_srd(const void* ptr, u32 bytes) {
  return v4u{(u32)(uintptr_t)ptr, (u32)((uintptr_t)ptr >> 32) & 0xffffu, bytes, 0x00020000u};
}

#define LDV(voff, off_s, i, aux) __builtin_amdgcn_raw_buffer_load_b128(rs, (voff) + (((i) & 3) << 10), (off_s) + (((i) >> 2) << 12), (aux))
#define LDW(off_s, i, aux) LDV(lane16, off_s, i, aux)
#define UNROLL _Pragma("unroll")
template <int B, class F, int... I>
__device__ __forceinline__ void sfor_impl(F&& f, std::integer_sequence<int, I...>) { (f(std::integral_constant<int, B + I>{}), ...); }
#define SFOR(i, B, E, ...) sfor_impl<B>([&](auto i##_) __attribute__((always_inline)) { constexpr int i = decltype(i##_)::value; __VA_ARGS__ }, std::make_integer_sequence<int, (E) - (B)>{})
constexpr u32 OOB = 0x80000000u;
constexpr u32 ONES = 0x3f803f80u;
constexpr f32x4 ZERO4 = {0.f, 0.f, 0.f, 0.f};
constexpr int QAF = 3;
constexpr int NE = 10, QS0 = 27, QSP = 5;
constexpr int NKT = 7, HH0 = 78, HHS = 3;
constexpr int WT0 = 30, WTS = 20;
constexpr int KZA = 10, KZB = 15, ZG = 2;
constexpr int NKEEP = 4, NKEEP7 = 16;
constexpr int W0S = 2, WSP_ = 4, BARS = 17;
constexpr int PSLEEP = 8, USLEEP = 30, PDELAY = 13, XDELAY = 10, TSLEEP = 3, DS_O = 9, DS_X = 3;
constexpr u32 CODEPF = 61440;
constexpr int NR_[] = {2, 3, 4, 4, 2, 3, 4, 3, 2}, NQ_[] = {5, 0, 4, 3, 4};
constexpr int R_TOP = NKT, R_ODR = R_TOP + NR_[0], R_OFL = R_ODR + NR_[1], R_P2 = R_OFL + NR_[2], R_CMB = R_P2 + NR_[3], R_XDR = R_CMB + NR_[4],
              R_XFL = R_XDR + NR_[5], R_PS1 = R_XFL + NR_[6], R_EPI = R_PS1 + 11, R_Z1A = R_EPI + NR_[7];
constexpr int Q_P4 = 0, Q_UFL = Q_P4 + NQ_[0], Q_P5 = Q_UFL + NQ_[1], Q_CMB = Q_P5 + NQ_[2], Q_XDR = Q_CMB + NQ_[3], Q_XFL = Q_XDR + NQ_[4];
constexpr int NWOUT7 = 6;
constexpr int RING = 8;

template <bool W7>
__device__ __forceinline__ void mega_body(const MegaArgs& a, LAS char* L, const int w_c, const u64 pc0) {
  const u32 lds0 = (u32)(uintptr_t)L;
  const int tid_c = threadIdx.x;
  const int b = blockIdx.x;
  const int p_c = map_p(b), m_c = map_m(b);
  int tid, w, p, m, lane, q, t, r, kh;
  u32 lane16, lane16i;
  LAS char *B0, *B1;
#define REFRESH() do { tid = tid_c; w = w_c; p = p_c; m = m_c; asm volatile("" : "+v"(tid)); asm volatile("" : "+s"(w), "+s"(p), "+s"(m)); if (W7) w = 7; lane = tid & 63; lane16 = lane * 16; \
    lane16i = (m >= 6 && w == 6) ? OOB : lane16; q = lane >> 4; t = lane & 15; r = w & 3; kh = w >> 2; } while (0)
#define BASES(off) BASESL(lane16, off)
#define BASESL(lo, off) do { u32 b0_ = lds0 + (lo) + (off), b1_ = b0_ + 65536; asm volatile("" : "+v"(b0_), "+v"(b1_)); B0 = (LAS char*)(uintptr_t)b0_; B1 = (LAS char*)(uintptr_t)b1_; } while (0)
#define FRAG(n) ((n) < 64 ? *(LAS v4u*)(B0 + (n) * 1024) : *(LAS v4u*)(B1 + ((n) - 64) * 1024))
  REFRESH();
  const __amdgpu_buffer_rsrc_t rs = __builtin_amdgcn_make_buffer_rsrc((void*)a.ws, 0, (int)WS_BYTES, 0x00020000);
  const __amdgpu_buffer_rsrc_t rs_out = __builtin_amdgcn_make_buffer_rsrc((void*)a.out, 0, S * D * 2, 0x00020000);
  const v4u srd = make_srd(a.ws, WS_BYTES);
#define wq0 (OFF_WQ + (m * 16 + 2 * w) * 20 * 1024)
#define wq1 (wq0 + 20 * 1024)
#define wi0 (OFF_WI + (m * 16 + 2 * w) * 20 * 1024)
#define wsp0 (OFF_WSP + (p * 8 + w) * 32 * 1024)
#define xoffP (((4 * p + r) * 20 + (d_pair(m, kh) >> 5)) * 1024)
#define xoffS (((4 * p + r) * 20 + (d_single(m) >> 5)) * 1024 + ((d_single(m) & 31) >> 3) * 256)
#define vS ((u32)(((q >> 1) * 16 + t) * 16 + (q & 1) * 8))

#define SX_FETCH(buf, rt0, NRT, fix) do { const u32 g_ = OFF_SX + (buf) * SXSZ + (rt0) * 2048; \
    SFOR(i, 0, 2 * (NRT), { dma16<1, (i & 3) * 1024>(srd, lane16, g_ + (i >> 2) * 4096 + (i < 2 ? (fix) : 0), lds0 + L_SX + (i >> 2) * 4096); }); } while (0)
#define SX_FINISH(NRT) do { UNROLL for (int h_ = 0; h_ < ((NRT) + 3) / 4; ++h_) { \
      LAS f32x2* sp_ = (LAS f32x2*)(L + L_SX + (h_ ? 4 : q) * 2048 + t * 8); \
      f32x2 v_[16]; \
      UNROLL for (int i = 0; i < 16; ++i) v_[i] = sp_[i * 16]; \
      SCHED(); \
      float sm_ = add1(v_[0].x, v_[1].x), sq_ = add1(v_[0].y, v_[1].y), sn_ = add1(v_[2].x, v_[3].x), sr_ = add1(v_[2].y, v_[3].y); \
      UNROLL for (int i = 4; i < 16; i += 2) { sm_ = add1(sm_, v_[i].x); sq_ = add1(sq_, v_[i].y); sn_ = add1(sn_, v_[i + 1].x); sr_ = add1(sr_, v_[i + 1].y); } \
      *(LAS f32x2*)(L + L_STAT + ((h_ ? 4 : q) * 16 + t) * 8) = ln_pair(add1(sm_, sn_), add1(sq_, sr_), 1.0f / D); \
      asm volatile("s_waitcnt lgkmcnt(0)" ::: "memory"); } } while (0)

  u32 zi, lane16z, redw, redt, z1t, redh, z1h, z1p, uA, uB, uC, wg4, wl4;
  float bias;
#define P4_PREP() do { zi = OFF_Z2 + it * ZSZ + (m * 7 * 64 + 8 * w) * 1024; lane16z = m < 6 ? lane16 : OOB; \
    redw = lds0 + w * 4096 + lane16; redh = lds0 + r * 1024 + lane16 + kh * 8; \
    z1h = lds0 + L_Z1 + r * 512 + lane * 8 + kh * 4; z1p = lds0 + L_Z1 + r * 512 + lane * 8 + kh * 4096; \
    redt = lds0 + r * 1024 + (kh * 32 + (lane & 31)) * 16 + (lane >> 5) * 16384; \
    z1t = lds0 + L_Z1 + 6 * 2048 + r * 512 + (kh * 32 + (lane & 31)) * 8 + (lane >> 5) * 4; \
    bias = *(LAS float*)(L + L_BIAS + (16 * r + t) * 4); \
    const u32 uo_ = OFF_U + it * AT27 + (4 * p + r) * 27 * 1024; \
    uA = lane16 + uo_ + (f_pair(m, kh) >> 5) * 1024; \
    uB = kh == 0 ? lane16 + uo_ + (f_pair(m, 2) >> 5) * 1024 : OOB; \
    uC = m < 6 ? uo_ + (f_single(m) >> 5) * 1024 + (((f_single(m) & 31) >> 3) + kh) * 256 + t * 16 + (q & 1) * 8 + (lane >> 5) * 4 : OOB; \
    wg4 = OFF_WOUT + (m * 135 + 32 + 4 * w) * 1024; wl4 = lds0 + L_XT + (32 + 4 * w) * 1024; \
    asm volatile("" : "+v"(lane16z), "+v"(redw), "+v"(redt), "+v"(z1t), "+v"(redh), "+v"(z1h), "+v"(z1p), "+v"(bias), "+v"(uA), "+v"(uB), "+v"(uC)); \
    asm volatile("" : "+s"(zi), "+s"(wg4), "+s"(wl4)); } while (0)
  u32 pjO, pjX, pjS, exw, exr, stP, stS, stX, mk1, mk2;
  f32x4 pc[5];
#define PROJ_PREP(KT, KS0, xbuf, sbuf) do { \
    pjO = lds0 + L_XT + lane16 + (2 * kh * (KT) + kh * (KS0)) * 1024; pjX = lds0 + L_XT + lane16 + (2 * (1 - kh) * (KT) + kh * (KS0)) * 1024; \
    pjS = lds0 + L_XT + lane16 + (4 * (KT) + kh * (KS0)) * 1024; \
    exw = lds0 + L_EXCH + lane16 + r * 5120 + kh * 2048; exr = lds0 + L_EXCH + lane16 + r * 5120 + (1 - kh) * 2048; \
    stP = lane16 + (xbuf) + xoffP; stS = kh ? OOB : vS + (xbuf) + xoffS; \
    stX = q == (t >> 2) ? OFF_SX + (sbuf) * SXSZ + ((4 * p + r) * 16 + 2 * m + kh) * 128 + t * 8 : OOB; mk1 = 0u - (t & 1); mk2 = 0u - ((t >> 1) & 1); \
    pc[0] = f32x4{bf2f(resP.x), bfhi(resP.x), bf2f(resP.y), bfhi(resP.y)}; pc[1] = f32x4{bf2f(resP.z), bfhi(resP.z), bf2f(resP.w), bfhi(resP.w)}; \
    pc[4] = kh ? ZERO4 : f32x4{bf2f(resS.x), bfhi(resS.x), bf2f(resS.y), bfhi(resS.y)}; \
    asm volatile("" : "+v"(pjO), "+v"(pjX), "+v"(pjS), "+v"(exw), "+v"(exr), "+v"(stP), "+v"(stS), "+v"(stX), "+v"(mk1), "+v"(mk2), "+v"(pc[0]), "+v"(pc[1]), "+v"(pc[4])); \
  } while (0)
#define LD128(base, off) (*(LAS v4u*)((LAS char*)(uintptr_t)(base) + (off)))
#define LDF4(base, off) (*(LAS f32x4*)((LAS char*)(uintptr_t)(base) + (off)))
#define PJT(i) (((i) % 5 + 2) % 5)
#define PJF(KT, i) LD128(PJT(i) < 2 ? pjO : PJT(i) < 4 ? pjX : pjS, ((PJT(i) & 1) * (PJT(i) < 4 ? (KT) : 0) + (i) / 5) * 1024)
#define PROJ_COMBINE(PRE) do { \
    LDF4(exw, 0) = pc[2]; LDF4(exw, 1024) = pc[3]; \
    if (kh) LDF4(exw, 2048) = pc[4]; \
    PRE; \
    BAR(); \
    const v2u a0_ = addpack(pc[0], LDF4(exr, 0)), a1_ = addpack(pc[1], LDF4(exr, 1024)); \
    resP = v4u{a0_.x, a0_.y, a1_.x, a1_.y}; \
    resS = addpack(pc[4], LDF4(exr, 2048)); } while (0)
#define PROJ_PUBLISH() do { \
    __builtin_amdgcn_raw_buffer_store_b128(resP, rs, stP, 0, SC1); \
    __builtin_amdgcn_raw_buffer_store_b64(resS, rs, stS, 0, SC1); \
    const v4u ones_ = {ONES, ONES, ONES, ONES}, sg_ = {kh ? 0u : resS.x, kh ? 0u : resS.y, 0u, 0u}; \
    f32x4 sm_ = mfma(ones_, resP, ZERO4), gr_ = mfma(resP, resP, ZERO4); \
    sm_ = mfma(ones_, sg_, sm_); gr_ = mfma(sg_, sg_, gr_); \
    const float d_ = bsel(mk2, bsel(mk1, gr_[3], gr_[2]), bsel(mk1, gr_[1], gr_[0])); \
    __builtin_amdgcn_raw_buffer_store_b64(v2u{ubits(sm_[0]), ubits(d_)}, rs, stX, 0, SC1); } while (0)

  v4u WH[NKT > 0 ? NKT : 1];
  v4u wt[4];
  v4u resP;
  v2u resS;
  u64 epoch = 0, sigv = 0;
  u32 sgP = 0, sgX = 0, sgZ = 0, plP = 0, plX = 0, plZ = 0;
  u64 wlo = 0, whi = 0;
#define SIG_PREP(ph) do { if constexpr (W7) { wlo = epoch + (ph); whi = epoch + 15; sigv = wlo; \
    asm volatile("" : "+s"(wlo), "+s"(whi)); asm volatile("" : "+v"(sigv)); } } while (0)
#define SIG_WAIT(sg, pl) do { signal(srd, sg, sigv); PAUSE(PDELAY); wait_phase(srd, pl, wlo, whi); } while (0)
  LAS char *B2, *B3;
#define XDMA(i) do { u32 o_ = o5[(i) % 5] + ((i) / 5) * 8192, g_ = g5[(i) % 5] + ((i) / 5) * 8192; \
    if ((i) == 12 && w >= 4) { o_ = o5[1] + 2 * 8192; g_ = g5[1] + 2 * 8192; } \
    dma16<1, 0>(srd, lane16, g_, o_); } while (0)
  u32 qw0, qw1, stp, sel0, sel1, sel2, sel3, rowp, rowq, trp, trq, oa, ob, mpar, mowa, mowb, mq0, nokey_, wgo, wlo8, wis, wss, wqs, lni, ln16;
#define ATT_PREP(itn) do { \
    qw0 = lds0 + L_QKV + t * QKV_STRIDE + (80 + 16 * w + 4 * q) * 2; \
    qw1 = lds0 + L_QKV + t * QKV_STRIDE + (((0xC432ED10u >> (4 * w)) & 15u) * 16 + 4 * q) * 2; \
    stp = lds0 + L_STAT + t * 8; \
    const int par_ = t & 1; \
    const bool oa_ = 4 * q + par_ <= t, ob_ = 4 * q + par_ + 2 <= t; \
    const u32 pa_ = par_ ? 0x01000c0cu : 0x0c0c0100u, pb_ = par_ ? 0x03020c0cu : 0x0c0c0302u; \
    sel0 = oa_ ? 0x0c0c0c0cu : pa_; sel1 = ob_ ? 0x0c0c0c0cu : pb_; sel2 = oa_ ? pa_ : 0x0c0c0c0cu; sel3 = ob_ ? pb_ : 0x0c0c0c0cu; \
    rowp = lds0 + L_QKV + (16 * (w >> 1) + t) * QKV_STRIDE + (w & 1) * 80 + 32;                       \
    rowq = rowp - 32 + q * 8;                                                                         \
    trq = lds0 + L_QKV + (16 * (w >> 1) + 4 * q + (t >> 2)) * QKV_STRIDE + (w & 1) * 80 + 320 + 32;   \
    trp = trq - 32 + (t & 3) * 8;                                                                     \
    oa = OFF_O + (itn) * AT20 + (4 * p + (w >> 1)) * 20480 + t * 16; \
    ob = q == 0 ? oa + (10 * m + 8 + (w & 1)) * 256 : OOB; \
    oa += (10 * m + 4 * (w & 1) + 2 * (q & 1) + (q >> 1)) * 256; \
    nokey_ = (p == 0 && w < 2) ? 0xf149f2cau : 0u;     \
    wgo = OFF_WO + m * 102400 + (12 + w) * 1024; wlo8 = lds0 + L_XT + (12 + w) * 1024;     \
    wis = wi0; wss = wsp0; wqs = wq0; lni = lane16i; ln16 = lane16; \
    asm volatile("" : "+s"(wis), "+s"(wss), "+s"(wqs), "+s"(wgo), "+s"(wlo8)); \
    asm volatile("" : "+v"(lni), "+v"(ln16), "+v"(nokey_)); \
    mpar = 0u - par_; mowa = oa_ ? ~0u : 0u; mowb = ob_ ? ~0u : 0u; mq0 = q == 0 ? ~0u : 0u; \
    asm volatile("" : "+v"(mpar), "+v"(mowa), "+v"(mowb), "+v"(mq0)); \
    asm volatile("" : "+v"(qw0), "+v"(qw1), "+v"(stp), "+v"(sel0), "+v"(sel1), "+v"(sel2), "+v"(sel3), "+v"(rowp), "+v"(rowq), "+v"(trp), "+v"(trq), "+v"(oa), "+v"(ob)); \
  } while (0)
#define RD64(base, off) (*(LAS v2u*)((LAS char*)(uintptr_t)(base) + (off)))
#define RDF2(base, off) (*(LAS f32x2*)((LAS char*)(uintptr_t)(base) + (off)))
#define RDTR(base, off) __builtin_bit_cast(v2u, __builtin_amdgcn_ds_read_tr16_b64_v4i16((LAS s16x4*)((LAS char*)(uintptr_t)(base) + (off))))
#define QN(i, d) (((i) - QS0 - (d) + QSP - 1) / QSP)
  static_assert(QS0 > 26 && QS0 + 3 < 36 && QN(60, 0) <= 20 - NE, "counted waits of the K loop");
#define WOA1(j) do { dma16<1, ((j) & 3) * 1024>(srd, lane16, OFF_WO + m * 102400 + ((j) / 12 * 20 + ((j) % 12 & ~3)) * 1024, lds0 + L_XT + ((j) / 12 * 20 + ((j) % 12 & ~3)) * 1024); } while (0)
#define P1KS(F, i) ((F) || (i) < 80 ? (i) / 5 : 16 + ((i) - 80) % 4)
#define P1RT(F, i) ((F) || (i) < 80 ? (i) % 5 : ((i) - 80) / 4)
#define P1_K(FIRST) do { \
    const bool halo1 = FIRST || w == 2 || w == 3;     \
    constexpr int QA = QAF;        \
    constexpr int NJ = W7 ? 1 : 2; \
    v4u x[100]; \
    f32x4 acc[2][5], cs[2], tt[2][5]; \
    f32x2 st[5]; \
    if constexpr (!W7) __builtin_amdgcn_s_setprio(1); \
    constexpr int LA = 4; \
    SFOR(i, 0, LA, { x[i] = FRAG((i % 5) * 20 + i / 5); }); \
    SFOR(i, 0, 100, { \
      constexpr int ks = P1KS(FIRST, i); constexpr int rt = P1RT(FIRST, i); \
      if constexpr (!FIRST && i == 16 - LA) BARV(3); \
      if constexpr (!FIRST && i == 40 - LA) BARV(W7 ? 5 + 10 + QN(40 - LA, 0) : 5 + QN(40 - LA, 0) + QN(40 - LA, 3)); \
      if constexpr (!FIRST && i == 64 - LA) BARVL(W7 ? QN(64 - LA, 0) : QN(64 - LA, 0) + QN(64 - LA, 3));     \
      if constexpr (FIRST && i == 80) { \
        if constexpr (W7) wait_phase(srd, plZ, wlo, whi); \
        BAR(); \
      } \
      mma<ks == 0>(acc[0][rt], Q0[ks], x[i]); \
      if constexpr (!FIRST && i + LA < 100) x[i + LA] = FRAG(P1RT(FIRST, i + LA) * 20 + P1KS(FIRST, i + LA)); \
      if constexpr (FIRST && i + 4 < 100) { constexpr int n_ = ((i + 4) % 5) * 20 + (i + 4) / 5; \
        x[i + 4] = (n_ & 1) ? (n_ < 64 ? *(LAS v4u*)(B2 + (n_ - 1) * 1024) : *(LAS v4u*)(B3 + (n_ - 65) * 1024)) : FRAG(n_); } \
      if constexpr (!W7 && rt > 0) mma<ks == 0>(acc[1][rt], Q1[ks], x[i]); \
      if constexpr (!W7 && rt == 0) { if (ks == 0 || halo1) mma<ks == 0>(acc[1][0], Q1[ks], x[i]); } \
      if constexpr (!FIRST && i < 2) XDMA(3 + i); \
      if constexpr (!FIRST && (i == 3 || i == 6 || i == 9)) XDMA(4 + i / 3); \
      if constexpr (!FIRST && i >= 13 && i <= 25 && i % 3 == 1) XDMA(8 + (i - 13) / 3); \
      if constexpr (!FIRST && W7 && i == 26) SX_FETCH(2, 4 * p - 1, 5, p == 0 ? 2048 : 0); \
      if constexpr (!FIRST && W7 && i == 44) { WAITV(QN(44, 0)); SX_FINISH(5); } \
      if constexpr (FIRST && !W7 && ks + QA < 20 && rt == 1) Q0[ks + QA] = LDW(wq0, ks + QA, SC1); \
      if constexpr (FIRST && !W7 && ks + QA < 20 && rt == 3) Q1[ks + QA] = LDW(wq1, ks + QA, SC1); \
      if constexpr (!FIRST && i >= QS0 && (i - QS0) % QSP == 0 && (i - QS0) / QSP < 20 - NE) Q0[NE + (i - QS0) / QSP] = LDV(ln16, wqs, NE + (i - QS0) / QSP, SC1); \
      if constexpr (!FIRST && !W7 && i >= QS0 + 3 && (i - QS0 - 3) % QSP == 0 && (i - QS0 - 3) / QSP < 20 - NE) Q1[NE + (i - QS0 - 3) / QSP] = LDV(ln16, wqs + 20480, NE + (i - QS0 - 3) / QSP, SC1); \
      constexpr int H0 = FIRST ? 86 : HH0, HS = FIRST ? (NKT > 7 ? 1 : 2) : HHS; \
      if constexpr (!W7 && i >= H0 && (i - H0) % HS == 0 && (i - H0) / HS < NKT) WH[(i - H0) / HS] = LDV(lni, wis, (i - H0) / HS, SC1); \
      constexpr int T0_ = W7 ? 5 : WT0, TS_ = W7 ? 10 : WTS; \
      if constexpr (FIRST && i >= T0_ && (i - T0_) % TS_ == 0 && (i - T0_) / TS_ < 4) \
        st128(wt[(i - T0_) / TS_], rs, lane16 + (i - T0_) / TS_ * 4096, OFF_WSP + ((p * 8 + m) * 32 + (w & 1) * 16 + (w >> 1)) * 1024); \
      if constexpr (FIRST && W7 && i == 80) pin(Q0[19]); \
      if constexpr (FIRST && W7 && i >= 80) WOA1(i - 80); \
        \
      if constexpr (i >= (FIRST ? 85 : 60) && i < (FIRST ? 90 : 65)) st[i % 5] = RDF2(stp, (i % 5) * 128); \
      if constexpr (!FIRST && i >= 50 && i < 50 + NJ) cs[i - 50] = *(LAS f32x4*)(L + L_CQ + w * 128 + (i - 50) * 64 + q * 16); \
      constexpr int T0 = 66; \
      if constexpr (!FIRST && i >= T0 && i < T0 + 10 * NJ) { constexpr int e_ = 2 * (i - T0), tr_ = e_ / (4 * NJ), tj_ = (e_ / 4) % NJ; \
        tt[tj_][tr_][e_ & 3] = mulv(st[tr_].x, cs[tj_][e_ & 3]); \
        tt[tj_][tr_][(e_ & 3) + 1] = mulv(st[tr_].x, cs[tj_][(e_ & 3) + 1]); } \
      if constexpr (!FIRST && i >= 86 && (i & 3) >= 2 && (i & 3) - 2 < NJ) { \
        if constexpr (W7) mfma_settle(); \
        RD64(((i & 3) - 2) ? qw1 : qw0, (rt - 1) * 16 * QKV_STRIDE) = fold4b(acc[(i & 3) - 2][rt - 1], tt[(i & 3) - 2][rt - 1], st[rt - 1].y); } \
      SCHED(); \
    }); \
    __builtin_amdgcn_s_setprio(0);     \
    mfma_settle(); \
    if constexpr (FIRST) { \
        \
      if (t == 0) { UNROLL for (int j = 0; j < NJ; ++j) *(LAS f32x4*)(L + L_CQ + w * 128 + j * 64 + q * 16) = acc[j][0]; } \
      asm volatile("s_waitcnt lgkmcnt(0)" ::: "memory"); \
      UNROLL for (int j = 0; j < NJ; ++j) cs[j] = *(LAS f32x4*)(L + L_CQ + w * 128 + j * 64 + q * 16); \
      UNROLL for (int j = 0; j < NJ; ++j) keepalive(cs[j]); \
    } \
    UNROLL for (int rt = (FIRST ? 0 : 4); rt < 5; ++rt) UNROLL for (int j = 0; j < NJ; ++j) \
      RD64(j ? qw1 : qw0, rt * 16 * QKV_STRIDE) = FIRST ? fold4(acc[j][rt], cs[j], st[rt]) : fold4b(acc[j][rt], tt[j][rt], st[rt].y); \
    if constexpr (W7) {     \
      __builtin_amdgcn_s_setprio(0); \
      SFOR(i, FIRST ? 20 : 0, 60, { WOA1(i); if constexpr (i == 40) WAITV(20); }); \
      __builtin_amdgcn_s_setprio(3); \
    } \
    } while (0)

  {
#define UNIT_FETCH(usrd, ldb, colb, k0, NCH, NDMA, PERM, stg) SFOR(i, 0, NDMA, { \
      const u32 c_ = 64 * i + lane, row_ = c_ / (NCH), k_ = (k0) + row_; \
      dma16<0, 0>(usrd, ((PERM) ? o_row(k_) : k_) * (ldb) + (colb) + (c_ - row_ * (NCH)) * 16, 0u, lds0 + (stg) + i * 1024); })
#define UNIT_FRAG(stg, rowb, cb, dst) do { \
      LAS char* sp_ = L + (stg) + (8 * q + (t >> 2)) * (rowb) + (cb); \
      const v2u a0_ = __builtin_bit_cast(v2u, __builtin_amdgcn_ds_read_tr16_b64_v4i16((LAS s16x4*)sp_)); \
      const v2u a1_ = __builtin_bit_cast(v2u, __builtin_amdgcn_ds_read_tr16_b64_v4i16((LAS s16x4*)(sp_ + 4 * (rowb)))); \
      st128(v4u{a0_.x, a0_.y, a1_.x, a1_.y}, rs, lane16, dst); } while (0)
    const v4u wsrd = make_srd(a.wsp, S * S * 2);
#define WSP_FETCH(j) SFOR(i, 0, 4, { dma16<2, 0>(wsrd, q * (S * 2) + (t ^ (4 * i + q)) * 16, \
      (64 * p + 16 * ((j) >> 1) + 4 * i) * (S * 2) + m * 512 + ((j) & 1) * 256, lds0 + L_STG + (j) * 4096 + i * 1024); })
    const u32 xlane = (t >> 3) * 1024 + (t & 7) * 128 + (q ^ (t >> 1)) * 16;
    v4u Q0[20], Q1[20];
    if constexpr (W7) {
      const int sec = (p * 205) >> 11, ks0 = 2 * (p - 10 * sec);
      const u64 ev = __hip_atomic_load((GAS u64*)(a.ws + (lane == 0 ? OFF_G : OFF_CNT + (p * 8 + m) * 512 + lane * 8)), __ATOMIC_RELAXED, __HIP_MEMORY_SCOPE_AGENT);
      const u32 bs = a.bsp[64 * p + lane];
      sgX = lane < (p < 31 ? 16 : 8) ? OFF_CNT + (p * 512 + m + (lane & 8) + lane * 64) * 8 : OOB;
      sgP = lane < 8 ? sgX : OOB;
      sgZ = lane < 32 ? OFF_CNT + (m * 64 + 16 + p + lane * 512) * 8 : OOB;
      plP = OFF_CNT + ((p * 8 + m) * 64 + (lane & 7)) * 8;
      plX = p > 0 ? plP + (lane & 8) * 8 : plP;
      plZ = OFF_CNT + ((p * 8 + m) * 64 + 16 + (lane & 31)) * 8;
      asm volatile("" : "+v"(sgP), "+v"(sgX), "+v"(sgZ), "+v"(plP), "+v"(plX), "+v"(plZ));
      WAITV(0);
      epoch = ((((u64)(u32)__builtin_amdgcn_readfirstlane((u32)(ev >> 32)) << 32) | (u32)__builtin_amdgcn_readfirstlane((u32)ev)) & ~15ull) + 16;
      SIG_PREP(1);
      *(LAS float*)(L + L_BIAS + lane * 4) = bf2f(bs);
      SCHED();
      if (p < 30) {
        const u32 slots = sec == 0 ? 0xdb931u : sec == 1 ? 0x86420u : 0x75ecau;
        SFOR(i, 0, 10, { UNIT_FRAG(L_STQ + (i / 5) * 5120, 160, (i % 5) * 32 + (t & 3) * 8,
                                   OFF_WQ + ((m * 16 + ((slots >> (4 * (i % 5))) & 15u)) * 20 + ks0 + i / 5) * 1024); });
      }
      WAITV(0);
      signal(srd, sgZ, sigv);
      WSP_FETCH(6); WSP_FETCH(7);
      ATT_PREP(0);
      wait_phase(srd, plZ, wlo, whi);
      asm volatile("s_barrier" ::: "memory");
      UNROLL for (int i = 0; i < 20; ++i) Q0[i] = LDW(wq0, i, SC1);
      SCHED();
      if (p < 20) {
        const u32 pb = (f_pair(m, 0) - f_base(m)) * 2 + (t & 3) * 16, sb = (f_single(m) - f_base(m)) * 2 + (t & 3) * 8;
        SFOR(i, 0, 14, { constexpr int c = i % 7;
          UNIT_FRAG(L_STU + (i / 7) * 7168, 224, c < 6 ? pb + (c >> 1) * 64 + (c & 1) * 8 : sb,
                    (c < 6 || m < 6) ? OFF_WI + ((m * 16 + 2 * c + i / 7) * 20 + p) * 1024 : OOB); });
      } else {
        const u32 pb = (d_pair(m, 0) - 80 * m) * 2 + (t & 3) * 16, sb = (d_single(m) - 80 * m) * 2 + (t & 3) * 8;
        const int u = (p - 20) * 4;
        const u32 d0 = u < 20 ? OFF_WO + (m * 100 + u) * 1024 : OFF_WOUT + (m * 135 + u - 20) * 1024, ds = u < 20 ? 20480 : 27648;
        SFOR(i, 0, 20, { constexpr int c = i % 5;
          UNIT_FRAG(L_STU + (i / 5) * 5120, 160, c < 4 ? pb + (c >> 1) * 64 + (c & 1) * 8 : sb, (i < 15 || p < 31) ? d0 + c * ds + (i / 5) * 1024 : OOB); });
      }
      SCHED();
      SIG_PREP(2);
      WAITV(0);
      signal(srd, sgZ, sigv);
    } else {
      dma16<0, 0>(make_srd((const void*)(pc0 & ~255ull), CODEPF), w < 2 ? lane16 + w * 1024 + (b >> 3) * 2048 : OOB, 0u, lds0 + L_CQ);
      const v4u xsrd = make_srd(a.x, S * D * 2);
      const u32 v0 = (lane >> 3) * (D * 2) + ((lane & 7) ^ (lane >> 4)) * 16, v1 = (v0 ^ 64) + 8 * (D * 2);
      const u32 g0 = ((p == 0 && w == 2) ? 0 : 4 * p - 3 + w) * 16 * (D * 2), l0 = lds0 + L_XT + (w - 2) * 20480;
      __builtin_amdgcn_s_sleep(PSLEEP);
      if (w >= 2) {
        u32 k2 = (m * 10) >> 3;
        SFOR(i, 0, 10, {
          dma16<0, 0>(xsrd, v0, g0 + k2 * 128, l0 + k2 * 2048);
          dma16<0, 0>(xsrd, v1, g0 + k2 * 128, l0 + k2 * 2048 + 1024);
          k2 = k2 == 9 ? 0 : k2 + 1;
        });
      } else {
        __builtin_amdgcn_s_sleep(USLEEP);
        if (p < 20) {
          const v4u usrd = make_srd(a.win, 640 * 3456); UNIT_FETCH(usrd, 3456, (f_base(m) + (w ? 0 : FH)) * 2, 32 * p, 14, 7, false, L_STU + w * 7168);
        } else {
          const int u = (p - 20) * 4 + 2 * w;
          if (u < 20) { const v4u usrd = make_srd(a.wo, 640 * 1280); UNIT_FETCH(usrd, 1280, 160 * m, 32 * u, 10, 10, true, L_STU + w * 10240); }
          else { const v4u usrd = make_srd(a.wout, FH * 1280); UNIT_FETCH(usrd, 1280, 160 * m, 32 * (u - 20), 10, 10, false, L_STU + w * 10240); }
        }
      }
      ATT_PREP(0);
      WAITV(0);
      if (w < 6) WSP_FETCH(w);
      if (w >= 2) {
        if (w == 2) {
          const v4u ones = {ONES, ONES, ONES, ONES};
          *(LAS v4u*)(L + L_XT + (lane >> 3) * 2048 + (lane & 7) * 16) = ones;
          *(LAS v4u*)(L + L_XT + (8 + ((lane >> 3) & 1)) * 2048 + (lane & 7) * 16) = ones;
          asm volatile("s_waitcnt lgkmcnt(0)" ::: "memory");
        }
        float s0 = 0.f, s1 = 0.f, q0 = 0.f, q1 = 0.f;
        v4u xv[20];
        UNROLL for (int i = 0; i < 20; ++i) xv[i] = *(LAS v4u*)(L + L_XT + ((w - 2) * 20 + (i & ~1)) * 1024 + (xlane ^ ((i & 1) * 64)));
        SCHED();
        UNROLL for (int i = 0; i < 20; ++i) {
          const v4u v = xv[i];
          s0 = dot2(v.x, ONES, s0); q0 = dot2(v.x, v.x, q0); s1 = dot2(v.y, ONES, s1); q1 = dot2(v.y, v.y, q1);
          s0 = dot2(v.z, ONES, s0); q0 = dot2(v.z, v.z, q0); s1 = dot2(v.w, ONES, s1); q1 = dot2(v.w, v.w, q1);
        }
        float sa_ = xsum32(xsum16(s0 + s1));
        asm volatile("" : "+v"(sa_));
        *(LAS f32x2*)(L + L_STAT + ((w - 2) * 16 + t) * 8) = ln_pair(sa_, xsum32(xsum16(q0 + q1)), 1.0f / D);
      }
      BARVL(0);
      UNROLL for (int i = 0; i < QAF; ++i) { Q0[i] = LDW(wq0, i, SC1); Q1[i] = LDW(wq1, i, SC1); }
      SCHED();
    }
    UNROLL for (int i = 0; i < 4; ++i) wt[i] = *(LAS v4u*)(L + L_STG + w * 4096 + t * 256 + ((4 * i + q) ^ t) * 16);
    {
      const int kp = d_pair(m, kh) >> 5, c8 = (d_single(m) >> 3) + (q >> 1);
      resP = *(LAS v4u*)(L + L_XT + ((r + 1) * 20 + (kp & ~1)) * 1024 + (xlane ^ ((kp & 1) * 64)));
      resS = *(LAS v2u*)(L + L_XT + ((r + 1) * 20 + (c8 >> 3) * 2 + (t >> 3)) * 1024 + (t & 7) * 128 + ((c8 & 7) ^ (t >> 1)) * 16 + (q & 1) * 8);
    }
    BASESL(xlane ^ 64, L_XT);
    B2 = B0; B3 = B1;
    BASESL(xlane, L_XT);
    SCHED();
    u32 o5[5], g5[5];
    P1_K(true);
  }

  constexpr int NK = W7 ? NKEEP7 : NKEEP;
  v4u WK[NK > 0 ? NK : 1];
  UNROLL for (int i = 0; i < NK; ++i) asm volatile("" : "=v"(WK[i]));
#pragma unroll 1
  for (int it = 0; it < 2; ++it) {
    v4u Q0[20], Q1[20];
    v4u WI[40];
    v4u WS[32];
#define REQ(n) do { if constexpr (!W7 && (n) < 40) WI[(n) % 40] = LDV(lni, wis, (n), SC1); \
      else if constexpr (!W7 && (n) < 72 - NKEEP) WS[((n) + 24) % 32] = LDV(ln16, wss, (n) - 40, SC1); \
      else if constexpr (!W7 && (n) < 72) { if (it == 0) WK[((n) + NKEEP - 72 + 64) % 64 % (NK > 0 ? NK : 1)] = LDV(ln16, wss, (n) - 40, SC1); } } while (0)
#define REQS(n0, n1) SFOR(i_, n0, n1, { REQ(i_); })
#define RQQ(n) do { if constexpr (!W7 && (n) < 2 * NE) { if constexpr ((n) & 1) Q1[(n) / 2 % 20] = LDV(ln16, wqs + 20480, (n) / 2, SC1); \
      else Q0[(n) / 2 % 20] = LDV(ln16, wqs, (n) / 2, SC1); } } while (0)
#define RQQS(n0, n1) SFOR(i_, n0, n1, { RQQ(i_); })
    {
      if constexpr (!W7) { UNROLL for (int i = 0; i < NKT; ++i) WI[i] = WH[i]; }
      REQS(R_TOP, R_ODR);
      SIG_PREP(4 * it + 1);
      const float nokey = fbits(nokey_);
      SCHED();
      BAR();
      v4u kf[2][2], qf[2], vf[2], vl[2];
      UNROLL for (int u = 0; u < 2; ++u) {
        const v2u ql = RD64(rowq, 16 * QKV_STRIDE + u * 40), qh = RD64(rowp, 16 * QKV_STRIDE + u * 40);
        qf[u] = v4u{ql.x, ql.y, qh.x & mq0, qh.y & mq0};
        asm volatile("" ::: "memory");
        UNROLL for (int j = 0; j < 2; ++j) {
          const v2u lo = RD64(rowq, j * 16 * QKV_STRIDE + 160 + u * 40), hi = RD64(rowp, j * 16 * QKV_STRIDE + 160 + u * 40);
          kf[u][j] = v4u{lo.x, lo.y, hi.x, hi.y};
          asm volatile("" ::: "memory");
        }
      }
      UNROLL for (int u = 0; u < 2; ++u) {
        const v2u a0 = RDTR(trp, u * 40), a1 = RDTR(trp, 16 * QKV_STRIDE + u * 40), b0 = RDTR(trq, u * 40), b1 = RDTR(trq, 16 * QKV_STRIDE + u * 40);
        vf[u] = v4u{a0.x, a0.y, a1.x, a1.y}; vl[u] = v4u{b0.x, b0.y, b1.x, b1.y};
      }
#define WOB(i) dma16<1, 0>(srd, ln16, wgo + (i) * 20480, wlo8 + (i) * 20480)
      WOB(0);
      f32x4 s0[2], s1[2];
      UNROLL for (int u = 0; u < 2; ++u) { s0[u] = mfma(kf[u][0], qf[u], ZERO4); s1[u] = mfma(kf[u][1], qf[u], ZERO4); }
      WOB(1);
      float sa[2], sb[2], mx[2];
      constexpr float SC = 0.22360679774997896f * 1.4426950408889634f;
      UNROLL for (int u = 0; u < 2; ++u) {
        float xa = bsel(mpar, s0[u][1], s0[u][0]), xb = bsel(mpar, s0[u][3], s0[u][2]);
        xa = add1(xa, nokey); xb = add1(xb, nokey);
        sa[u] = bsel(mowa, bsel(mpar, s1[u][1], s1[u][0]), xa);
        sb[u] = bsel(mowb, bsel(mpar, s1[u][3], s1[u][2]), xb);
        mx[u] = max1(sa[u], sb[u]);
      }
      SCHED(); WOB(2);
      {
        auto r0 = __builtin_amdgcn_permlane16_swap(ubits(mx[0]), ubits(mx[1]), false, false);
        const float m1 = max1(fbits(r0[0]), fbits(r0[1]));
        auto r1 = __builtin_amdgcn_permlane32_swap(ubits(m1), ubits(m1), false, false);
        const float m2 = max1(fbits(r1[0]), fbits(r1[1])) * SC;
        auto r2 = __builtin_amdgcn_permlane16_swap(ubits(m2), ubits(m2), false, false);
        mx[0] = fbits(r2[0]); mx[1] = fbits(r2[1]);
      }
      SCHED(); WOB(3);
      f32x4 oA[2], oB[2], den[2];
      const v4u ones = {ONES, ONES, ONES, ONES};
      UNROLL for (int u = 0; u < 2; ++u) {
        const u32 c = pack2(__builtin_amdgcn_exp2f(__builtin_fmaf(sa[u], SC, -mx[u])), __builtin_amdgcn_exp2f(__builtin_fmaf(sb[u], SC, -mx[u])));
        const v4u pf = {__builtin_amdgcn_perm(c, c, sel0), __builtin_amdgcn_perm(c, c, sel1), __builtin_amdgcn_perm(c, c, sel2), __builtin_amdgcn_perm(c, c, sel3)};
        den[u] = mfma(ones, pf, ZERO4);
        oA[u] = mfma(vf[u], pf, ZERO4);
        oB[u] = mfma(vl[u], pf, ZERO4);
      }
      WOB(4);
      {
        const float i0 = __builtin_amdgcn_rcpf(den[0][0]), i1 = __builtin_amdgcn_rcpf(den[1][0]);
        UNROLL for (int e = 0; e < 4; ++e) {
          float y0 = oA[0][e] * i0, y1 = oA[1][e] * i1;
          asm volatile("" : "+v"(y0)); asm volatile("" : "+v"(y1));
          oA[0][e] = y0; oA[1][e] = y1;
        }
        auto x0 = __builtin_amdgcn_permlane16_swap(pack2(oA[0][0], oA[0][1]), pack2(oA[1][0], oA[1][1]), false, false);
        auto x1 = __builtin_amdgcn_permlane16_swap(pack2(oA[0][2], oA[0][3]), pack2(oA[1][2], oA[1][3]), false, false);
        __builtin_amdgcn_raw_buffer_store_b128(v4u{x0[0], x1[0], x0[1], x1[1]}, rs, oa, 0, SC1);
        UNROLL for (int e = 0; e < 4; ++e) {
          float y0 = oB[0][e] * i0, y1 = oB[1][e] * i1;
          asm volatile("" : "+v"(y0)); asm volatile("" : "+v"(y1));
          oB[0][e] = y0; oB[1][e] = y1;
        }
        __builtin_amdgcn_raw_buffer_store_b128(v4u{pack2(oB[0][0], oB[0][1]), pack2(oB[0][2], oB[0][3]), pack2(oB[1][0], oB[1][1]), pack2(oB[1][2], oB[1][3])}, rs, ob, 0, SC1);
      }
      SCHED();
      PAUSE(DS_O);
      REQS(R_ODR, R_OFL);
      SCHED();
      u32 ao;
#define P2_PREP() do { REFRESH(); PROJ_PREP(20, 10, OFF_X1 + it * AT20, it); ao = OFF_O + it * AT20 + ((4 * p + r) * 20 + 10 * kh) * 1024; asm volatile("" : "+s"(ao)); } while (0)
      if constexpr (W7) P2_PREP();
      BARV(W7 ? 0 : R_OFL - R_ODR);
      if constexpr (W7) {
        SIG_WAIT(sgP, plP);
      } else {
        REQS(R_OFL, R_P2);
        P2_PREP();
      }
      asm volatile("s_barrier" ::: "memory");
      v4u A[10], Bf[50];
      UNROLL for (int i = 0; i < 5; ++i) A[i] = LDW(ao, i, SC1);
      SFOR(i, 0, 8, { Bf[i] = PJF(20, i); });
      SIG_PREP(4 * it + 2);
      SCHED();
      SFOR(i, 0, 50, {
        pc[PJT(i)] = mfma(Bf[i], A[i / 5], (i < 2) ? ZERO4 : pc[PJT(i)]);
        if constexpr (i + 8 < 50) Bf[i + 8] = PJF(20, i + 8);
        if constexpr (i % 5 == 0 && i < 25) A[i / 5 + 5] = LDW(ao, i / 5 + 5, SC1);
        if constexpr (i % 5 == 2 && i / 5 >= 10 - (R_CMB - R_P2)) REQ(R_CMB - 10 + i / 5);
        SCHED();
      });
      PROJ_COMBINE(REQS(R_CMB, R_XDR));
      PROJ_PUBLISH();
      SCHED();
      REFRESH();
      PAUSE(DS_X);
      REQS(R_XDR, R_XFL);
      SCHED();
    }
    f32x4 acc1[4], cs1;
    f32x2 st[4];
    float c1;
    u32 z1w;
    {
      u32 xl = w * 1024, xg = OFF_X1 + it * AT20 + 4 * p * 20 * 1024 + xl;
      xl += lds0 + L_XT;
      asm volatile("" : "+s"(xl), "+s"(xg));
#define X1DMA(i) dma16<1, 0>(srd, lane16, xg + (i) * 8192, xl + (i) * 8192)
#define X1F(i) FRAG(((i) & 3) * 20 + ((i) >> 2))
      BARV(W7 ? 0 : R_XFL - R_XDR);
      if constexpr (W7) {
        SIG_WAIT(sgP, plP);
        asm volatile("s_barrier" ::: "memory");
        X1DMA(0); X1DMA(1); X1DMA(2);
        BARV(1);
        X1DMA(3);
        SX_FETCH(it, 4 * p, 4, 0);
        X1DMA(4); X1DMA(5);
        BARV(10);
        X1DMA(6); X1DMA(7); X1DMA(8);
        WAITV(5);
        SX_FINISH(4);
        BARVL(3);
        X1DMA(9);
        BARV(2);
        BARV(0);
        __builtin_amdgcn_s_setprio(0);
        UNROLL for (int i = 0; i < 16; ++i) WS[i] = LDW(wsp0, i, SC1);
        {
          const u32 wg = OFF_WOUT + (m * 135 + 80) * 1024, wl = lds0 + L_XT + 80 * 1024;
          SFOR(i, 0, NWOUT7, { dma16<1, (i & 3) * 1024>(srd, lane16, wg + (i >> 2) * 4096, wl + (i >> 2) * 4096); });
        }
        __builtin_amdgcn_s_setprio(3);
        u32 sto = OFF_ST + ((it * 32 + p) * 8) * 1024;
        u32 stw = lane16 + sto + m * 1024, prt = lds0 + L_PART + lane * 8;
        u32 tag0 = (u32)(epoch + 1), tag1 = (u32)(epoch >> 32) ^ tag0 ^ 0x9e3779b9u;
        asm volatile("" : "+s"(sto));
        asm volatile("" : "+v"(stw), "+v"(prt), "+v"(tag0), "+v"(tag1));
        SIG_PREP(it + 3);
        WAITV(0);
        asm volatile("s_barrier" ::: "memory");
        f32x2 pt[7];
        UNROLL for (int i = 0; i < 7; ++i) pt[i] = RDF2(prt, i * 512);
        const f32x2 sm = f32x2{add1(add1(add1(pt[0].x, pt[1].x), add1(pt[2].x, pt[3].x)), add1(add1(pt[4].x, pt[5].x), pt[6].x)),
                               add1(add1(add1(pt[0].y, pt[1].y), add1(pt[2].y, pt[3].y)), add1(add1(pt[4].y, pt[5].y), pt[6].y))};
        __builtin_amdgcn_raw_buffer_store_b128(v4u{ubits(sm.x), tag0, ubits(sm.y), tag1}, rs, stw, 0, SC1);
        PAUSE(XDELAY);
        u32 spins = 0;
        float ts, tq;
#pragma unroll 1
        while (true) {
          v4u g[8];
          UNROLL for (int i = 0; i < 8; ++i) g[i] = LDW(sto, i, SC1);
          u32 bad = 0u;
          UNROLL for (int i = 0; i < 8; ++i) bad |= (g[i].y ^ tag0) | (g[i].w ^ tag1);
          ts = add1(add1(add1(fbits(g[0].x), fbits(g[1].x)), add1(fbits(g[2].x), fbits(g[3].x))), add1(add1(fbits(g[4].x), fbits(g[5].x)), add1(fbits(g[6].x), fbits(g[7].x))));
          tq = add1(add1(add1(fbits(g[0].z), fbits(g[1].z)), add1(fbits(g[2].z), fbits(g[3].z))), add1(add1(fbits(g[4].z), fbits(g[5].z)), add1(fbits(g[6].z), fbits(g[7].z))));
          if (__all(bad == 0u)) break;
          if (++spins > SPIN_CAP) break;
          asm volatile("" ::: "memory");
        }
        RDF2(prt, L_FIN - L_PART) = ln_pair(ts, tq, 1.0f / FH);
        BAR();
        __builtin_amdgcn_s_setprio(0);
        PAUSE(8);
        static_assert(NKEEP7 == 16, "");
        if (it == 0) { UNROLL for (int i = 16; i < 32; ++i) WK[i - 16] = LDW(wsp0, i, SC1); }
        __builtin_amdgcn_s_setprio(3);
        P4_PREP();
        SCHED();
        asm volatile("s_barrier" ::: "memory");
        SIG_WAIT(sgZ, plZ);
      } else {
        REQS(R_XFL, R_PS1);
        constexpr int NCS = R_XFL < 20 ? R_XFL : 20;
        static_assert(R_PS1 >= 20 && R_PS1 <= 40, "z2 weights are requested ahead of the pass, w_sp behind the x1 flag");
        SCHED();
        const v4u ones = {ONES, ONES, ONES, ONES};
        f32x4 cs;
        SFOR(i, 0, NCS, { cs = mfma(WI[i], ones, i ? cs : ZERO4); });
        BASES(L_XT);
        u32 sp = lds0 + L_STAT + t * 8, prw = lds0 + L_PART + w * 512 + lane * 8;
        u32 trw = lds0 + L_TR + w * 2048 + t * 32 + q * 8, trr = lds0 + L_TR + w * 2048 + (8 * q + (t >> 2)) * 32 + (t & 3) * 8;
        u32 zo = (w < 6 || m < 6) ? lane16 + OFF_Z2 + it * ZSZ + ((m * 7 + w) * 64 + 2 * p) * 1024 : OOB;
        c1 = -2.30220820f;
        asm volatile("" : "+v"(sp), "+v"(prw), "+v"(trw), "+v"(trr), "+v"(zo), "+v"(c1));
        SCHED();
        asm volatile("s_barrier" ::: "memory");
        f32x4 acc[4];
        X1DMA(0); X1DMA(1); X1DMA(2);
        BARV(1);
        float z[4][4], ps[4], pq[4], ge[4];
        static_assert(NCS == 20, "the column sums are complete in front of the pass");
        {
          v4u x[80];
          UNROLL for (int i = 0; i < 4; ++i) x[i] = FRAG(i);
          SFOR(i, 0, 80, {
            constexpr int ks = i % 20; constexpr int rt = i / 20;
            if constexpr (i == 12) BARV(2);
            if constexpr (i == 28) BARV(3);
            if constexpr (i == 44) BARV(4);
            if constexpr (i == 60) BARV(6);
            mma<ks == 0>(acc[rt], WI[ks], x[i]);
            if constexpr (i + 4 < 80) x[i + 4] = FRAG(i + 4);
            if constexpr (i == 0) X1DMA(3);
            if constexpr (i == 4) X1DMA(4);
            if constexpr (i == 8) X1DMA(5);
            if constexpr (i == 13) X1DMA(6);
            if constexpr (i == 18) X1DMA(7);
            if constexpr (i == 23) X1DMA(8);
            if constexpr (i == 29) X1DMA(9);
            if constexpr (i >= 37 && (i & 3) == 1) REQ(R_PS1 + (i - 37) / 4);
            if constexpr (i >= 28 && i < 32) st[i - 28] = RDF2(sp, (i - 28) * 128);
            constexpr int j = (i - 32) & 15; constexpr int tl = (i - 32) >> 4;
            if constexpr (i >= 32 && j % 3 != 2) gelu_row<j - j / 3>(acc[tl], cs, st[tl], c1, z[tl], ge, ps[tl], pq[tl]);
            SCHED();
          });
        }
        gelu_row<0>(acc[3], cs, st[3], c1, z[3], ge, ps[3], pq[3]);
        asm volatile("s_nop 3");
        SFOR(k, 1, 11, {
          gelu_row<k>(acc[3], cs, st[3], c1, z[3], ge, ps[3], pq[3]);
          if constexpr (k <= NR_[7]) REQ(R_EPI + k - 1);
        });
        asm volatile("s_nop 1");
        {
          float s2[2], q2[2];
          UNROLL for (int i = 0; i < 2; ++i) {
            auto r0 = __builtin_amdgcn_permlane32_swap(ubits(ps[i]), ubits(ps[2 + i]), false, false);
            s2[i] = fbits(r0[0]) + fbits(r0[1]);
            asm volatile("" : "+v"(s2[i]));
            auto r1 = __builtin_amdgcn_permlane32_swap(ubits(pq[i]), ubits(pq[2 + i]), false, false);
            q2[i] = fbits(r1[0]) + fbits(r1[1]);
            asm volatile("" : "+v"(q2[i]));
          }
          auto r0 = __builtin_amdgcn_permlane16_swap(ubits(s2[0]), ubits(s2[1]), false, false);
          auto r1 = __builtin_amdgcn_permlane16_swap(ubits(q2[0]), ubits(q2[1]), false, false);
          RDF2(prw, 0) = f32x2{add1(fbits(r0[0]), fbits(r0[1])), add1(fbits(r1[0]), fbits(r1[1]))};
        }
        BAR();
#define P3_Z1(k0, k1, n0) do { \
          v4u x[80]; \
          UNROLL for (int i = 4 * (k0); i < 4 * (k0) + 4; ++i) x[i] = X1F(i); \
          SFOR(i, 4 * (k0), 4 * (k1), { \
            mma<(i < 4)>(acc1[i & 3], WI[20 + (i >> 2)], x[i]); \
            if constexpr (i + 4 < 4 * (k1)) x[i + 4] = X1F(i + 4); \
            if constexpr ((i & 3) == 3) cs1 = mfma(WI[20 + (i >> 2)], ones, i >= 4 ? cs1 : ZERO4); \
            if constexpr ((i & 3) == 1) REQ((n0) + (i >> 2) - (k0)); \
            SCHED(); \
          }); } while (0)
        P3_Z1(0, KZA, R_Z1A);
        constexpr int R_NRM = R_Z1A + KZA, R_Z1B = R_NRM + NR_[8], R_Z1C = R_Z1B + KZB - KZA;
        REFRESH();
        P4_PREP();
        asm volatile("s_barrier" ::: "memory");
        f32x2 fs[4];
        UNROLL for (int rt = 0; rt < 4; ++rt) { fs[rt] = RDF2(sp, L_FIN - L_STAT + rt * 128); asm volatile("" ::: "memory"); }
        SCHED();
        REQS(R_NRM, R_Z1B);
        asm volatile("" : "+v"(fs[0]), "+v"(fs[1]), "+v"(fs[2]), "+v"(fs[3]));
        UNROLL for (int rt = 0; rt < 4; ++rt) { RD64(trw, rt * 512) = norm4(z[rt], fs[rt]); asm volatile("" ::: "memory"); }
        asm volatile("s_waitcnt lgkmcnt(0)" ::: "memory");
        UNROLL for (int k2 = 0; k2 < 2; ++k2) {
          const v2u lo = RDTR(trr, k2 * 1024), hi = RDTR(trr, k2 * 1024 + 128);
          __builtin_amdgcn_raw_buffer_store_b128(v4u{lo.x, lo.y, hi.x, hi.y}, rs, zo + k2 * 1024, 0, SC1);
        }
        SCHED();
        static_assert(R_Z1C <= 72 - NKEEP, "the counted wait below assumes real requests");
        P3_Z1(KZA, KZB, R_Z1B);
        BARV(KZB - KZA);
        P3_Z1(KZB, 20, R_Z1C);
        REFRESH();
        z1w = lds0 + L_Z1 + w * 2048 + lane * 8;
        asm volatile("" : "+v"(z1w));
        mfma_settle();
        constexpr int NREST = R_Z1C + 20 - KZB;
        SFOR(rt, 0, 4, {
          SFOR(j, 0, 3, { REQ(NREST + 3 * rt + j); });
          if constexpr (rt < 4 - ZG) {
            float g[4];
            gelu4(acc1[rt], cs1, st[rt], c1, g);
            RD64(z1w, rt * 512) = v2u{pack2(g[0], g[1]), pack2(g[2], g[3])};
          }
          SCHED();
        });
        static_assert(NREST + 12 >= 72, "every fragment is requested");
        {
          constexpr int K7 = (55 - NWOUT7) / 7;
          const u32 wg = OFF_WOUT + (m * 135 + 80 + NWOUT7 + K7 * w) * 1024, wl = lds0 + L_XT + (80 + NWOUT7 + K7 * w) * 1024;
          SFOR(i, 0, K7, { dma16<1, (i & 3) * 1024>(srd, lane16, wg + (i >> 2) * 4096, wl + (i >> 2) * 4096); });
        }
      }
    }
    {
      SCHED();
      BAR();
      v4u Z[56];
#define ZLD(f) Z[f] = LDV((f) >= 48 ? lane16z : lane16, zi + ((f) >> 3) * 65536, (f) & 7, SC1)
      SFOR(k, 0, 4, {
        SFOR(f, k == 0 ? 0 : 1 + 2 * k, k == 3 ? RING : 3 + 2 * k, { ZLD(f); SCHED(); });
        if constexpr (!W7 && k < ZG) {
          float g[4];
          gelu4(acc1[4 - ZG + k], cs1, st[4 - ZG + k], c1, g);
          RD64(z1w, (4 - ZG + k) * 512) = v2u{pack2(g[0], g[1]), pack2(g[2], g[3])};
        }
        SCHED();
      });
      static_assert(RING >= 7, "");
      f32x4 acc[2][4];
      f32x2 part[8];
      u32 z1;
      SIG_PREP(4 * it + 3);
      SCHED();
      float sa = 0.f, sb = 0.f;
      v2u plo, phi;
      SFOR(n, 0, 224, {
        constexpr int f = n >> 2; constexpr int rt = n & 3; constexpr int c = f >> 3; constexpr int ks = f & 7; constexpr int s_ = n & 31;
        mma<ks == 0>(acc[c & 1][rt], Z[f], ks * 4 + rt < 32 - NK ? WS[ks * 4 + rt] : WK[(ks * 4 + rt + NK) % 32 % (NK > 0 ? NK : 1)]);
        if constexpr (rt == 3 && f + RING < 56) ZLD(f + RING);
        if constexpr (rt == 3 && f >= 56 - Q_UFL) RQQ(f - 56 + Q_UFL);
        if constexpr (rt == 3 && W7 && f >= 53) Q0[f - 53] = LDV(ln16, wqs, f - 53, SC1);
        if constexpr (c > 0) {
          if constexpr (s_ >= W0S && s_ < W0S + 4 * WSP_ && (s_ - W0S) % WSP_ == 0) LDF4(redw, ((c - 1) & 1) * 32768 + (s_ - W0S) / WSP_ * 1024) = acc[(c - 1) & 1][(s_ - W0S) / WSP_];
          if constexpr (s_ == BARS) BAR();
          if constexpr (s_ > BARS && s_ <= BARS + 8) { part[s_ - BARS - 1] = RDF2(redh, ((c - 1) & 1) * 32768 + (s_ - BARS - 1) * 4096); fence(); }
          if constexpr (s_ == BARS + 9) z1 = *(LAS u32*)(uintptr_t)(z1h + (c - 1) * 2048);
          if constexpr (c == 5 && s_ == BARS + 10) { plo = RD64(z1p, 0); fence(); phi = RD64(z1p, 2048); }
          if constexpr (s_ == BARS + 5) { sa = addv(part[0].x, bias); sb = addv(part[0].y, bias); }
          if constexpr (s_ > BARS + 5 && s_ <= BARS + 12) { sa = addv(sa, part[s_ - BARS - 5].x); sb = addv(sb, part[s_ - BARS - 5].y); }
          if constexpr (s_ == BARS + 13) { sa = mulv(bf2f(z1), sa); sb = mulv(bfhi(z1), sb); }
          if constexpr (c == 5 && s_ == BARS + 14) { __builtin_amdgcn_raw_buffer_store_b128(v4u{plo.x, plo.y, phi.x, phi.y}, rs, uA, 0, SC1); fence(); }
          if constexpr (s_ == BARS + 14) *(LAS u32*)(uintptr_t)(z1h + (c - 1) * 2048) = pack2(sa, sb);
        }
        SCHED();
      });
      static_assert(BARS + 14 < 32 && W0S + 3 * WSP_ < BARS, "schedule of a round");
      dma16<1, 0>(srd, ln16, wg4 + 32768 - 2048 * w_c, wl4 + 32768 - 2048 * w_c); dma16<1, 1024>(srd, ln16, wg4 + 32768 - 2048 * w_c, wl4 + 32768 - 2048 * w_c);
      mfma_settle();
      UNROLL for (int rt = 0; rt < 4; ++rt) LDF4(redw, rt * 1024) = acc[0][rt];
      BAR();
      {
        f32x4 pt[4];
        UNROLL for (int i = 0; i < 4; ++i) pt[i] = LDF4(redt, i * 4096);
        const u32 zz = *(LAS u32*)(uintptr_t)z1t;
        const v2u lo = RD64(z1p, 4 * 2048), hi = RD64(z1p, 5 * 2048);
        float sum[4];
        sum4x4(pt, sum);
        auto r0 = __builtin_amdgcn_permlane32_swap(ubits(sum[0]), ubits(sum[2]), false, false);
        auto r1 = __builtin_amdgcn_permlane32_swap(ubits(sum[1]), ubits(sum[3]), false, false);
        __builtin_amdgcn_raw_buffer_store_b128(v4u{lo.x, lo.y, hi.x, hi.y}, rs, uB, 0, SC1);
        __builtin_amdgcn_raw_buffer_store_b32(pack2(mul1(bf2f(zz), add1(add1(fbits(r0[0]), fbits(r0[1])), bias)),
                                                    mul1(bfhi(zz), add1(add1(fbits(r1[0]), fbits(r1[1])), bias))), rs, uC, 0, SC1);
      }
      SCHED();
      PAUSE(TSLEEP);
      SFOR(i, 0, 4, { dma16<1, i * 1024>(srd, ln16, wg4, wl4); });
    }
    {
      u32 ao, lane16k, pjL;
#define P5_PREP() do { REFRESH(); PROJ_PREP(27, 14, OFF_X2, 2); \
      ao = OFF_U + it * AT27 + ((4 * p + r) * 27 + 14 * kh) * 1024; lane16k = kh ? OOB : lane16; pjL = pjS + (13 - kh) * 1024; \
      if (it == 1) {     \
        const u32 row = (64 * p + 16 * r + t) * (D * 2); \
        stP = row + (d_pair(m, kh) + 8 * q) * 2; \
        stS = kh ? OOB : row + (d_single(m) + 4 * q) * 2; \
      } \
      asm volatile("" : "+s"(ao)); \
      asm volatile("" : "+v"(lane16k), "+v"(pjL), "+v"(stP), "+v"(stS)); } while (0)
      if constexpr (W7) P5_PREP();
      BARVL(4);
      if constexpr (W7) {
        SIG_WAIT(sgP, plP);
      } else {
        const u32 g = OFF_WOUT + (m * 135 + 5 * w) * 1024, l = lds0 + L_XT + 5 * w * 1024;
        dma16<1, 0>(srd, lane16, g, l); dma16<1, 1024>(srd, lane16, g, l);
        if (w < 6) { dma16<1, 2048>(srd, lane16, g, l); dma16<1, 3072>(srd, lane16, g, l); dma16<1, 0>(srd, lane16, g + 4096, l + 4096); }
        SCHED();
        RQQS(Q_UFL, Q_P5);
        SCHED();
        P5_PREP();
        SCHED();
        WAITV(Q_P5 - Q_UFL);
      }
      asm volatile("s_barrier" ::: "memory");
      v4u A[14], Bf[70];
      UNROLL for (int i = 0; i < 5; ++i) A[i] = LDW(ao, i, SC1);
      SFOR(i, 0, 8, { Bf[i] = PJF(27, i); });
      SIG_PREP(4);
      SCHED();
      SFOR(i, 0, 70, {
        pc[PJT(i)] = mfma(Bf[i], A[i / 5], (i < 2) ? ZERO4 : pc[PJT(i)]);
        if constexpr (i + 8 < 70 && i + 8 != 67) Bf[i + 8] = PJF(27, i + 8);
        if constexpr (i + 8 == 67) Bf[67] = LD128(pjL, 0);
        if constexpr (i % 5 == 0 && i < 40) A[i / 5 + 5] = LDW(ao, i / 5 + 5, SC1);
        if constexpr (i == 40) A[13] = LDV(lane16k, ao, 13, SC1);
        if constexpr (i % 5 == 2 && i / 5 >= 14 - (Q_CMB - Q_P5)) RQQ(Q_CMB - 14 + i / 5);
        if constexpr (W7 && i % 5 == 2 && i / 5 >= 11) Q0[i / 5 - 8] = LDV(ln16, wqs, i / 5 - 8, SC1);
        SCHED();
      });
      PROJ_COMBINE(RQQS(Q_CMB, Q_XDR));
      if (it == 1) {
        __builtin_amdgcn_raw_buffer_store_b128(resP, rs_out, stP, 0, 0);
        __builtin_amdgcn_raw_buffer_store_b64(resS, rs_out, stS, 0, 0);
        if (W7 && b == 0 && lane == 0) __hip_atomic_store((GAS u64*)(a.ws + OFF_G), epoch, __ATOMIC_RELAXED, __HIP_MEMORY_SCOPE_AGENT);
        asm volatile("s_endpgm" ::: "memory");
      }
      PROJ_PUBLISH();
      SCHED();
      REFRESH();
      PAUSE(DS_X);
      RQQS(Q_XDR, Q_XFL);
      if constexpr (W7) { UNROLL for (int i = 6; i < NE; ++i) Q0[i] = LDW(wq0, i, SC1); }
      u32 o5[5], g5[5];
#define P1_PREP() do { UNROLL for (int i = 0; i < 5; ++i) { \
        const int n = w + 8 * i, ks = (n * 205) >> 10, rt = n - 5 * ks; \
        o5[i] = lds0 + L_XT + (rt * 20 + ks) * 1024; \
        g5[i] = OFF_X2 + (((p == 0 && rt == 0) ? 0 : 4 * p - 1 + rt) * 20 + ks) * 1024;     \
        asm volatile("" : "+s"(o5[i]), "+s"(g5[i])); } \
      BASES(L_XT); ATT_PREP(1); } while (0)
      if constexpr (W7) P1_PREP();
      SCHED();
      BARV(W7 ? NE - 6 : Q_XFL - Q_XDR);
      if constexpr (W7) {
        SIG_WAIT(sgX, plX);
      } else {
        RQQS(Q_XFL, 2 * NE);
        P1_PREP();
      }
      SCHED();
      asm volatile("s_barrier" ::: "memory");
      XDMA(0); XDMA(1); XDMA(2);
      BARV(1);
      P1_K(false);
    }
  }
  __builtin_amdgcn_s_waitcnt(0);
}

__global__ void __launch_bounds__(512, 2) mega_kernel(MegaArgs a) {
  extern __shared__ __attribute__((aligned(16))) char smem[];
  u64 pc;
  asm volatile("s_getpc_b64 %0" : "=s"(pc));
  LAS char* L = (LAS char*)smem;
  const int w = __builtin_amdgcn_readfirstlane(threadIdx.x >> 6);
  {
    const int b = blockIdx.x, p = map_p(b), m = map_m(b), sec = (p * 205) >> 11;
    const v4u usrd = make_srd(a.wqkv, 640 * 3840);
    const u32 so = 64 * (p - 10 * sec) * 3840 + (sec * 640 + 80 * m) * 2, lds0 = (u32)(uintptr_t)L;
    if (w == 7 && p < 30) {
      SFOR(i, 0, 5, {
        const u32 c_ = 64 * i + (threadIdx.x & 63), row_ = (c_ * 6554) >> 16, vo_ = row_ * (3840 - 160) + c_ * 16;
        dma16<0, 0>(usrd, vo_, so, lds0 + L_STQ + i * 1024);
        dma16<0, 0>(usrd, vo_, so + 32 * 3840, lds0 + L_STQ + 5120 + i * 1024);
      });
    }
  }
  int w2 = w;
  asm volatile("" : "+s"(w2));
  if (w2 == 7) {
    __builtin_amdgcn_s_setprio(3);
    mega_body<true>(a, L, w, pc);
  } else {
    mega_body<false>(a, L, w, pc);
  }
}

}

extern "C" void kernel_launch(void* const* d_in, const int* in_sizes, int n_in, void* d_out, int out_size, void* d_ws,
                              size_t ws_size, hipStream_t stream) {
  MegaArgs ma;
  ma.ws = (char*)d_ws;
  ma.x = (const u16*)d_in[0];
  ma.wqkv = (const u16*)d_in[1];
  ma.wo = (const u16*)d_in[2];
  ma.win = (const u16*)d_in[4];
  ma.wsp = (const u16*)d_in[5];
  ma.bsp = (const u16*)d_in[6];
  ma.wout = (const u16*)d_in[7];
  ma.out = (u16*)d_out;
  (void)hipFuncSetAttribute((const void*)mega_kernel, hipFuncAttributeMaxDynamicSharedMemorySize, LDS_BYTES);
  mega_kernel<<<256, 512, LDS_BYTES, stream>>>(ma);
}
```
